```python
import torch

def _softmax(x, axis=-1):
    return torch.softmax(x, dim=axis)
def _gelu(x):
    return torch.nn.functional.gelu(x, approximate="tanh")
def _silu(x):
    return torch.nn.functional.silu(x)
def _sigmoid(x):
    return torch.sigmoid(x)
def _layernorm(x, axis=-1, eps=1e-5):
    m = x.mean(dim=axis, keepdim=True)
    v = x.var(dim=axis, keepdim=True, unbiased=False)
    return (x - m) / torch.sqrt(v + eps)
def _scatter_add(tgt, idx, src, axis=0):
    return tgt.clone().index_add_(axis, idx, src)
def _segment_sum(src, seg, n):
    out = torch.zeros((n,) + src.shape[1:], dtype=src.dtype)
    return out.index_add_(0, seg, src)
def _pad(x, pw):
    flat = tuple(v for pair in reversed(pw) for v in pair)
    return torch.nn.functional.pad(x, flat)

kernel_name = 'parallel_sum__sliding_block_cached__dense_activation_gelu__decode'

def setup_inputs():

    _w = (lambda a: (lambda t: t.to(torch.bfloat16) if t.is_floating_point() else t)(torch.as_tensor(a)))
    def _A_setup():
        B, Sq, D = 8, 1, 512
        Hq, Hkv, Sk, Dh = 32, 8, 4096, 128
        assert Hq % Hkv == 0, (Hq, Hkv)
        return {
            "x": _w((lambda *s: torch.randn(*s, dtype=torch.bfloat16) * 0.05)(B, Sq, D)),
            "k_cache": _w((lambda *s: torch.randn(*s, dtype=torch.bfloat16) * 3.2)(B, Hkv, Sk, Dh)),
            "v_cache": _w((lambda *s: torch.randn(*s, dtype=torch.bfloat16) * 0.8)(B, Hkv, Sk, Dh)),
            "wq": _w((lambda *s: torch.randn(*s, dtype=torch.bfloat16) * 3.2)(D, Hq * Dh) * (D ** -0.5)),
            "wo": _w((lambda *s: torch.randn(*s, dtype=torch.bfloat16) * 0.8)(Hq * Dh, D) * ((Hq * Dh) ** -0.5)),
        }
    _A_in = _A_setup()

    def _B_setup():
        B, S, D, F = 8, 1, 512, 1792
        return {
            "x": _w((lambda *s: torch.randn(*s, dtype=torch.bfloat16) * 0.05)(B, S, D)),
            "w1": _w((lambda *s: torch.randn(*s, dtype=torch.bfloat16) * 3.2)(D, F) * (D ** -0.5)),
            "b1": _w((lambda *s: torch.randn(*s, dtype=torch.bfloat16) * 3.2)(F) * 0.01),
            "w2": _w((lambda *s: torch.randn(*s, dtype=torch.bfloat16) * 0.4)(F, D) * (F ** -0.5)),
            "b2": _w((lambda *s: torch.randn(*s, dtype=torch.bfloat16) * 0.4)(D) * 0.01),
        }
    _B_in = _B_setup()

    return {
        "A_x": _A_in["x"],
        "A_k_cache": _A_in["k_cache"],
        "A_v_cache": _A_in["v_cache"],
        "A_wq": _A_in["wq"],
        "A_wo": _A_in["wo"],
        "B_x": _B_in["x"],
        "B_w1": _B_in["w1"],
        "B_b1": _B_in["b1"],
        "B_w2": _B_in["w2"],
        "B_b2": _B_in["b2"],
    }

def reference(A_x, A_k_cache, A_v_cache, A_wq, A_wo, B_x, B_w1, B_b1, B_w2, B_b2):
    def _A_ref(x, k_cache, v_cache, wq, wo):
        B, Sq, D = x.shape
        Hq, Dh = 32, 128
        Hkv, Sk = k_cache.shape[1], k_cache.shape[2]
        W = min(4096, max(256, Sk // 4))
        rep = Hq // Hkv
        q = (lambda x, *d: x.permute(*d))((x @ wq).reshape(B, Sq, Hq, Dh), 0, 2, 1, 3)
        k = (lambda x, n, axis: torch.repeat_interleave(x, n, dim=axis))(k_cache[:, :, Sk - W:, :], rep, axis=1)
        v = (lambda x, n, axis: torch.repeat_interleave(x, n, dim=axis))(v_cache[:, :, Sk - W:, :], rep, axis=1)
        scores = torch.einsum("bhqd,bhkd->bhqk", q, k) * (Dh ** -0.5)
        p = _softmax(scores, axis=-1)
        o = torch.einsum("bhqk,bhkd->bhqd", p, v)
        return (lambda x, *d: x.permute(*d))(o, 0, 2, 1, 3).reshape(B, Sq, Hq * Dh) @ wo

    def _B_ref(x, w1, b1, w2, b2):
        h = torch.einsum("bsd,df->bsf", x, w1) + b1
        h = _gelu(h)
        return torch.einsum("bsf,fd->bsd", h, w2) + b2

    x = A_x
    xn = _layernorm(x, axis=-1)
    return x + _A_ref(xn, A_k_cache, A_v_cache, A_wq, A_wo) + _B_ref(xn, B_w1, B_b1, B_w2, B_b2)
```

```cpp
#include <hip/hip_runtime.h>
#include <hip/hip_bf16.h>
#include <stdint.h>

namespace {

constexpr int NB = 8, DM = 512, NHKV = 8, SK = 4096, DH = 128, WINDOW = 1024, FF = 1792;
constexpr int NWG = 256, NTHR = 512;
constexpr int NQC = 4096;
constexpr int N_HWG = FF / 16;
constexpr int N_KC = 32 + FF / 128;
constexpr int N_CWG = N_KC * 4;

typedef unsigned v4u __attribute__((ext_vector_type(4)));
typedef unsigned v2u __attribute__((ext_vector_type(2)));
typedef float f32x4 __attribute__((ext_vector_type(4)));
typedef float f32x2 __attribute__((ext_vector_type(2)));
typedef __bf16 bf16x2 __attribute__((ext_vector_type(2)));
typedef __attribute__((address_space(1))) unsigned gu32;

constexpr unsigned WS_E = 0;
constexpr unsigned WS_Q = 4096;
constexpr unsigned WS_H = WS_Q + 8 * 1024 * 16;
constexpr unsigned WS_O = WS_H + 8 * 448 * 16;
constexpr unsigned WS_ML = WS_O + 32 * 8 * 4 * 64 * 16;
constexpr unsigned WS_P = WS_ML + 32 * 8 * 4 * 16;
constexpr unsigned WS_END = WS_P + N_KC * 8 * 256 * 16;

constexpr unsigned SPIN_CAP = 1u << 18;

__device__ __forceinline__ float bf2f(unsigned bits16) { return __builtin_bit_cast(float, bits16 << 16); }
__device__ __forceinline__ float bflo(unsigned w) { return __builtin_bit_cast(float, w << 16); }
__device__ __forceinline__ float bfhi(unsigned w) { return __builtin_bit_cast(float, w & 0xffff0000u); }
__device__ __forceinline__ unsigned f2bf(float f) {
  return (unsigned)__builtin_bit_cast(unsigned short, (__bf16)f);
}
__device__ __forceinline__ float bfr(float f) { return bf2f(f2bf(f)); }
__device__ __forceinline__ unsigned pack2(float lo, float hi) { return f2bf(lo) | (f2bf(hi) << 16); }
__device__ __forceinline__ float dot2(unsigned a, unsigned b, float acc) {
  return __builtin_amdgcn_fdot2_f32_bf16(__builtin_bit_cast(bf16x2, a), __builtin_bit_cast(bf16x2, b), acc, false);
}
template <int CTRL> __device__ __forceinline__ float dpp(float x) {
  return __builtin_bit_cast(float, __builtin_amdgcn_mov_dpp(__builtin_bit_cast(int, x), CTRL, 0xf, 0xf, true));
}
constexpr int XOR1 = 0xB1, XOR2 = 0x4E, XOR7 = 0x141, XOR8 = 0x128, ROR4 = 0x124;
__device__ __forceinline__ float xrow_max(float x) {
  auto s = __builtin_amdgcn_permlane16_swap(__float_as_uint(x), __float_as_uint(x), false, false);
  x = fmaxf(__uint_as_float(s[0]), __uint_as_float(s[1]));
  auto t = __builtin_amdgcn_permlane32_swap(__float_as_uint(x), __float_as_uint(x), false, false);
  return fmaxf(__uint_as_float(t[0]), __uint_as_float(t[1]));
}
__device__ __forceinline__ float xrow_sum(float x) {
  auto s = __builtin_amdgcn_permlane16_swap(__float_as_uint(x), __float_as_uint(x), false, false);
  x = __uint_as_float(s[0]) + __uint_as_float(s[1]);
  auto t = __builtin_amdgcn_permlane32_swap(__float_as_uint(x), __float_as_uint(x), false, false);
  return __uint_as_float(t[0]) + __uint_as_float(t[1]);
}
__device__ __forceinline__ float wave_sum(float x) {
#pragma unroll
  for (int m = 1; m < 64; m <<= 1) x += __shfl_xor(x, m);
  return x;
}
__device__ __forceinline__ v4u ldnt(const void* p) { return __builtin_nontemporal_load(reinterpret_cast<const v4u*>(p)); }

__device__ __forceinline__ void put_granule(__amdgpu_buffer_rsrc_t rs, unsigned off, unsigned w0, unsigned w1,
                                            unsigned tagA, unsigned tagB) {
  v4u g = {w0, tagA, w1, tagB};
  __builtin_amdgcn_raw_buffer_store_b128(g, rs, (int)off, 0, 16  );
}
template <int N>
__device__ __forceinline__ void get_granules(__amdgpu_buffer_rsrc_t rs, const unsigned (&off)[N], v4u (&g)[N],
                                             unsigned tagA, unsigned tagB) {
  for (unsigned spins = 0;; ++spins) {
    asm volatile("" ::: "memory");
    bool ok = true;
#pragma unroll
    for (int n = 0; n < N; ++n) g[n] = __builtin_amdgcn_raw_buffer_load_b128(rs, (int)off[n], 0, 16  );
#pragma unroll
    for (int n = 0; n < N; ++n) ok = ok && (g[n].y == tagA) && (g[n].w == tagB);
    if (__all(ok) || spins > SPIN_CAP) break;
    __builtin_amdgcn_s_sleep(1);
  }
  asm volatile("" ::: "memory");
}

__device__ __forceinline__ void pair_dot(v4u r0, v4u r1, const unsigned* act_col  , int act_stride,
                                         float (&acc)[64]) {
  unsigned pr[8];
#pragma unroll
  for (int w = 0; w < 4; ++w) {
    pr[2 * w] = __builtin_amdgcn_perm(r1[w], r0[w], 0x05040100u);
    pr[2 * w + 1] = __builtin_amdgcn_perm(r1[w], r0[w], 0x07060302u);
  }
#pragma unroll
  for (int b = 0; b < 8; ++b) {
    const unsigned a = act_col[b * act_stride];
#pragma unroll
    for (int j = 0; j < 8; ++j) acc[b * 8 + j] = dot2(pr[j], a, acc[b * 8 + j]);
  }
}
template <int NV, int M>
__device__ __forceinline__ void tr_step(float* a, int lane) {
  const bool up = (lane & M) != 0;
#pragma unroll
  for (int v = 0; v < NV / 2; ++v) {
    const float send = up ? a[v] : a[v + NV / 2];
    const float keep = up ? a[v + NV / 2] : a[v];
    a[v] = keep + __shfl_xor(send, M);
  }
}

struct __attribute__((aligned(16))) Lds {
  unsigned xnp[8][256];
  float redA[2][8][128];
  float pbuf[8][4][32];
  float rec[8][4][132];
  unsigned act[8][64];
  float redC[8][8][128];
  float part[N_KC][16];
};

__global__ void __launch_bounds__(NTHR, 2)
fused_decode_kernel(const unsigned short* __restrict__ x, const unsigned short* __restrict__ kc_,
                    const unsigned short* __restrict__ vc_, const unsigned short* __restrict__ wq,
                    const unsigned short* __restrict__ wo, const unsigned short* __restrict__ w1,
                    const unsigned short* __restrict__ b1, const unsigned short* __restrict__ w2,
                    const unsigned short* __restrict__ b2, unsigned short* __restrict__ out,
                    unsigned char* __restrict__ ws) {
  __shared__ Lds L;
  const int tid = threadIdx.x, lane = tid & 63;
  const int wave = __builtin_amdgcn_readfirstlane(tid >> 6);
  const int wg = blockIdx.x;
  const __amdgpu_buffer_rsrc_t rs = __builtin_amdgcn_make_buffer_rsrc((void*)ws, 0, (int)WS_END, 0x00020000);

  const unsigned epoch = __hip_atomic_load((gu32*)(ws + WS_E), __ATOMIC_RELAXED, __HIP_MEMORY_SCOPE_AGENT) + 1u;
  const v4u xv = *reinterpret_cast<const v4u*>(x + wave * DM + lane * 8);
  const int a_oct = lane >> 5, a_rp = wave * 32 + (lane & 31);
  const v4u wq0 = ldnt(wq + (size_t)(2 * a_rp) * NQC + wg * 16 + a_oct * 8);
  const v4u wq1 = ldnt(wq + (size_t)(2 * a_rp + 1) * NQC + wg * 16 + a_oct * 8);
  const __amdgpu_buffer_rsrc_t rsW1 =
      __builtin_amdgcn_make_buffer_rsrc((void*)w1, 0, wg < N_HWG ? DM * FF * 2 : 0, 0x00020000);
  const v4u w10 = __builtin_amdgcn_raw_buffer_load_b128(rsW1, ((2 * a_rp) * FF + wg * 16 + a_oct * 8) * 2, 0, 2);
  const v4u w11 = __builtin_amdgcn_raw_buffer_load_b128(rsW1, ((2 * a_rp + 1) * FF + wg * 16 + a_oct * 8) * 2, 0, 2);
  const __amdgpu_buffer_rsrc_t rsB1 =
      __builtin_amdgcn_make_buffer_rsrc((void*)b1, 0, wg < N_HWG ? FF * 2 : 0, 0x00020000);
  const v2u b1v = __builtin_amdgcn_raw_buffer_load_b64(rsB1, (wg * 16 + (tid & 3) * 4) * 2, 0, 0);
  const int d_n = (wg & 31) * 16 + (tid & 15);
  const unsigned d_b2 = b2[d_n];
  const unsigned d_x = x[(wg >> 5) * DM + d_n];
  __builtin_amdgcn_sched_barrier(0);
  const int ab = wg >> 5, kvh = (wg >> 2) & 7, sp = wg & 3;
  const int r = lane >> 4, c = lane & 15;
  v4u Kr[8], Vr[8];
  {
    const size_t base = ((size_t)(ab * NHKV + kvh) * SK + (SK - WINDOW) + sp * 256 + wave * 32 + r) * DH + c * 8;
#pragma unroll
    for (int i = 0; i < 8; ++i) Kr[i] = ldnt(kc_ + base + (size_t)i * 4 * DH);
#pragma unroll
    for (int i = 0; i < 8; ++i) Vr[i] = ldnt(vc_ + base + (size_t)i * 4 * DH);
  }
  __builtin_amdgcn_sched_barrier(0);
  const int ckc = wg >> 2, cnc = wg & 3;
  const int c_oct = tid & 15, c_rpg = tid >> 4;
  v4u Wc[4];
  {
    const unsigned short* wb = (ckc < 32) ? (wo + (size_t)ckc * 128 * DM) : (w2 + (size_t)(ckc - 32) * 128 * DM);
    const __amdgpu_buffer_rsrc_t rsC =
        __builtin_amdgcn_make_buffer_rsrc((void*)wb, 0, wg < N_CWG ? 128 * DM * 2 : 0, 0x00020000);
#pragma unroll
    for (int i = 0; i < 4; ++i)
      Wc[i] = __builtin_amdgcn_raw_buffer_load_b128(
          rsC, ((2 * (c_rpg + 32 * (i >> 1)) + (i & 1)) * DM + cnc * 128 + c_oct * 8) * 2, 0, 2);
  }
  __builtin_amdgcn_sched_barrier(0);
  const unsigned tagA = epoch, tagB = epoch ^ 0x9E3779B9u;

  {
    float xf[8];
#pragma unroll
    for (int w = 0; w < 4; ++w) { xf[2 * w] = bflo(xv[w]); xf[2 * w + 1] = bfhi(xv[w]); }
    float s = 0.f;
#pragma unroll
    for (int j = 0; j < 8; ++j) s += xf[j];
    const float mean = wave_sum(s) * (1.0f / DM);
    float q = 0.f;
#pragma unroll
    for (int j = 0; j < 8; ++j) { const float d = xf[j] - mean; q += d * d; }
    const float var = wave_sum(q) * (1.0f / DM);
    const float mb = bfr(mean);
    const float den = bfr(sqrtf(bfr(bfr(var) + 1e-5f)));
    v4u o;
#pragma unroll
    for (int w = 0; w < 4; ++w)
      o[w] = pack2(bfr(xf[2 * w] - mb) / den, bfr(xf[2 * w + 1] - mb) / den);
    *reinterpret_cast<v4u*>(&L.xnp[wave][lane * 4]) = o;
  }
  __syncthreads();

  {
    float acc[64];
#pragma unroll
    for (int i = 0; i < 64; ++i) acc[i] = 0.f;
    pair_dot(wq0, wq1, &L.xnp[0][a_rp], 256, acc);
    tr_step<64, 16>(acc, lane); tr_step<32, 8>(acc, lane); tr_step<16, 4>(acc, lane);
    tr_step<8, 2>(acc, lane); tr_step<4, 1>(acc, lane);
    *reinterpret_cast<f32x2*>(&L.redA[0][wave][lane * 2]) = f32x2{acc[0], acc[1]};
    if (wg < N_HWG) {
#pragma unroll
      for (int i = 0; i < 64; ++i) acc[i] = 0.f;
      pair_dot(w10, w11, &L.xnp[0][a_rp], 256, acc);
      tr_step<64, 16>(acc, lane); tr_step<32, 8>(acc, lane); tr_step<16, 4>(acc, lane);
      tr_step<8, 2>(acc, lane); tr_step<4, 1>(acc, lane);
      *reinterpret_cast<f32x2*>(&L.redA[1][wave][lane * 2]) = f32x2{acc[0], acc[1]};
    }
  }
  __syncthreads();
  if (tid < 64) {
    const int which = tid >> 5, t = tid & 31;
    const int b = t >> 2, quad = t & 3;
    if (which == 0 || wg < N_HWG) {
      const int idx = (quad >> 1) * 64 + b * 8 + (quad & 1) * 4;
      f32x4 s = *reinterpret_cast<const f32x4*>(&L.redA[which][0][idx]);
#pragma unroll
      for (int w = 1; w < 8; ++w) s += *reinterpret_cast<const f32x4*>(&L.redA[which][w][idx]);
      if (which == 0) {
        put_granule(rs, WS_Q + (unsigned)(b * 1024 + wg * 4 + quad) * 16u, pack2(s[0], s[1]), pack2(s[2], s[3]), tagA, tagB);
      } else {
        float hv[4];
#pragma unroll
        for (int j = 0; j < 4; ++j) {
          const float bj = (j & 1) ? bfhi(b1v[j >> 1]) : bflo(b1v[j >> 1]);
          const float h = bfr(bfr(s[j]) + bj);
          const float u = 0.7978845608028654f * (h + 0.044715f * h * h * h);
          hv[j] = 0.5f * h * (1.0f + tanhf(u));
        }
        put_granule(rs, WS_H + (unsigned)(b * 448 + wg * 4 + quad) * 16u, pack2(hv[0], hv[1]), pack2(hv[2], hv[3]), tagA, tagB);
      }
    }
  }

  {
    unsigned qoff[8];
    v4u qg[8];
#pragma unroll
    for (int g = 0; g < 4; ++g)
#pragma unroll
      for (int e = 0; e < 2; ++e) qoff[g * 2 + e] = WS_Q + (unsigned)(ab * 1024 + kvh * 128 + g * 32 + c * 2 + e) * 16u;
    get_granules<8>(rs, qoff, qg, tagA, tagB);
    float xs[32];
#pragma unroll
    for (int i = 0; i < 8; ++i)
#pragma unroll
      for (int g = 0; g < 4; ++g) {
        float d = dot2(Kr[i][0], qg[2 * g].x, 0.f);
        d = dot2(Kr[i][1], qg[2 * g].z, d);
        d = dot2(Kr[i][2], qg[2 * g + 1].x, d);
        d = dot2(Kr[i][3], qg[2 * g + 1].z, d);
        xs[i * 4 + g] = d;
      }
#define TR_STEP(HALF, CTRL, BIT)                                                         \
  _Pragma("unroll") for (int h = 0; h < (HALF); ++h) _Pragma("unroll") for (int j = 0; j < 2; ++j) { \
    const float lo_ = xs[h * 2 + j], hi_ = xs[(h + (HALF)) * 2 + j];                     \
    const float t1_ = lo_ + dpp<CTRL>(lo_), t2_ = hi_ + dpp<CTRL>(hi_);                  \
    xs[h * 2 + j] = (c & (BIT)) ? t2_ : t1_; }
    TR_STEP(8, XOR8, 8) TR_STEP(4, XOR7, 4) TR_STEP(2, XOR2, 2) TR_STEP(1, XOR1, 1)
#undef TR_STEP
    float p[2], mw[2], lw[2];
#pragma unroll
    for (int j = 0; j < 2; ++j) {
      const float s = bfr(bfr(xs[j]) * 0.08838834764831845f) * 1.4426950408889634f;
      float m = s;
      m = fmaxf(m, dpp<ROR4>(m)); m = fmaxf(m, dpp<XOR8>(m)); m = fmaxf(m, dpp<XOR2>(m)); m = xrow_max(m);
      p[j] = __builtin_amdgcn_exp2f(s - m);
      float l = p[j];
      l += dpp<ROR4>(l); l += dpp<XOR8>(l); l += dpp<XOR2>(l); l = xrow_sum(l);
      mw[j] = m; lw[j] = l;
    }
    float* prow = &L.pbuf[wave][r][0];
    *reinterpret_cast<f32x2*>(prow + c * 2) = f32x2{p[0], p[1]};
    __builtin_amdgcn_fence(__ATOMIC_RELEASE, "wavefront");
    __builtin_amdgcn_wave_barrier();
    __builtin_amdgcn_fence(__ATOMIC_ACQUIRE, "wavefront");
    float acc[32];
#pragma unroll
    for (int k = 0; k < 32; ++k) acc[k] = 0.f;
#pragma unroll
    for (int i = 0; i < 8; ++i) {
      const f32x4 pr = *reinterpret_cast<const f32x4*>(prow + i * 4);
      float vf[8];
#pragma unroll
      for (int w = 0; w < 4; ++w) { vf[2 * w] = bflo(Vr[i][w]); vf[2 * w + 1] = bfhi(Vr[i][w]); }
#pragma unroll
      for (int g = 0; g < 4; ++g)
#pragma unroll
        for (int k = 0; k < 8; ++k) acc[g * 8 + k] = fmaf(pr[g], vf[k], acc[g * 8 + k]);
    }
#pragma unroll
    for (int k = 0; k < 32; ++k) acc[k] = xrow_sum(acc[k]);
    if (r == 0) {
#pragma unroll
      for (int g = 0; g < 4; ++g) {
        float* rc = &L.rec[wave][g][0];
        *reinterpret_cast<f32x4*>(rc + c * 8) = f32x4{acc[g * 8], acc[g * 8 + 1], acc[g * 8 + 2], acc[g * 8 + 3]};
        *reinterpret_cast<f32x4*>(rc + c * 8 + 4) = f32x4{acc[g * 8 + 4], acc[g * 8 + 5], acc[g * 8 + 6], acc[g * 8 + 7]};
        if (c == (g >> 1)) { rc[128] = mw[g & 1]; rc[129] = lw[g & 1]; }
      }
    }
  }
  __syncthreads();
  if (tid < 256) {
    const int g = tid >> 6, dp = tid & 63;
    float M = L.rec[0][g][128];
#pragma unroll
    for (int w = 1; w < 8; ++w) M = fmaxf(M, L.rec[w][g][128]);
    float Ls = 0.f, o0 = 0.f, o1 = 0.f;
#pragma unroll
    for (int w = 0; w < 8; ++w) {
      const float wt = __builtin_amdgcn_exp2f(L.rec[w][g][128] - M);
      const f32x2 a = *reinterpret_cast<const f32x2*>(&L.rec[w][g][dp * 2]);
      Ls = fmaf(wt, L.rec[w][g][129], Ls); o0 = fmaf(wt, a[0], o0); o1 = fmaf(wt, a[1], o1);
    }
    const unsigned slot = (unsigned)(((kvh * 4 + g) * 8 + ab) * 4 + sp);
    put_granule(rs, WS_O + (slot * 64u + dp) * 16u, __float_as_uint(o0), __float_as_uint(o1), tagA, tagB);
    if (dp == 0) put_granule(rs, WS_ML + slot * 16u, __float_as_uint(M), __float_as_uint(Ls), tagA, tagB);
  }

  if (wg < N_CWG) {
    if (ckc < 32) {
      unsigned off[8];
      v4u g[8];
#pragma unroll
      for (int s = 0; s < 4; ++s) {
        const unsigned slot = (unsigned)((ckc * 8 + wave) * 4 + s);
        off[s] = WS_O + (slot * 64u + lane) * 16u;
        off[4 + s] = WS_ML + slot * 16u;
      }
      get_granules<8>(rs, off, g, tagA, tagB);
      float M = __uint_as_float(g[4].x);
#pragma unroll
      for (int s = 1; s < 4; ++s) M = fmaxf(M, __uint_as_float(g[4 + s].x));
      float Ls = 0.f, o0 = 0.f, o1 = 0.f;
#pragma unroll
      for (int s = 0; s < 4; ++s) {
        const float wt = __builtin_amdgcn_exp2f(__uint_as_float(g[4 + s].x) - M);
        Ls = fmaf(wt, __uint_as_float(g[4 + s].z), Ls);
        o0 = fmaf(wt, __uint_as_float(g[s].x), o0);
        o1 = fmaf(wt, __uint_as_float(g[s].z), o1);
      }
      L.act[wave][lane] = pack2(o0 / Ls, o1 / Ls);
    } else if (lane < 32) {
      unsigned off[1] = {WS_H + (unsigned)(wave * 448 + (ckc - 32) * 32 + lane) * 16u};
      v4u g[1];
      get_granules<1>(rs, off, g, tagA, tagB);
      L.act[wave][lane * 2] = g[0].x;
      L.act[wave][lane * 2 + 1] = g[0].z;
    }
    __syncthreads();
    float acc[64];
#pragma unroll
    for (int i = 0; i < 64; ++i) acc[i] = 0.f;
    pair_dot(Wc[0], Wc[1], &L.act[0][c_rpg], 64, acc);
    pair_dot(Wc[2], Wc[3], &L.act[0][c_rpg + 32], 64, acc);
    tr_step<64, 32>(acc, lane);
    tr_step<32, 16>(acc, lane);
    {
      const int bb = ((lane >> 5) << 2) | (((lane >> 4) & 1) << 1);
#pragma unroll
      for (int hb = 0; hb < 2; ++hb) {
        float* dst = &L.redC[wave][bb + hb][c_oct * 8];
        *reinterpret_cast<f32x4*>(dst) = f32x4{acc[hb * 8], acc[hb * 8 + 1], acc[hb * 8 + 2], acc[hb * 8 + 3]};
        *reinterpret_cast<f32x4*>(dst + 4) = f32x4{acc[hb * 8 + 4], acc[hb * 8 + 5], acc[hb * 8 + 6], acc[hb * 8 + 7]};
      }
    }
    __syncthreads();
    {
      f32x2 s = *reinterpret_cast<const f32x2*>(&L.redC[0][wave][lane * 2]);
#pragma unroll
      for (int w = 1; w < 8; ++w) s += *reinterpret_cast<const f32x2*>(&L.redC[w][wave][lane * 2]);
      put_granule(rs, WS_P + (unsigned)((ckc * 8 + wave) * 256 + cnc * 64 + lane) * 16u,
                  __float_as_uint(s[0]), __float_as_uint(s[1]), tagA, tagB);
    }
  }

  {
    const int db = wg >> 5, dg0 = (wg & 31) * 8;
    if (tid < N_KC * 8) {
      const int kc = tid >> 3, gq = tid & 7;
      unsigned off[1] = {WS_P + (unsigned)((kc * 8 + db) * 256 + dg0 + gq) * 16u};
      v4u g[1];
      get_granules<1>(rs, off, g, tagA, tagB);
      *reinterpret_cast<f32x2*>(&L.part[kc][gq * 2]) = f32x2{__uint_as_float(g[0].x), __uint_as_float(g[0].z)};
    }
    __syncthreads();
    if (tid < 16) {
      const int n = dg0 * 2 + tid;
      float sa = 0.f, sm = 0.f;
#pragma unroll
      for (int k = 0; k < 32; ++k) sa += L.part[k][tid];
#pragma unroll
      for (int k = 32; k < N_KC; ++k) sm += L.part[k][tid];
      const float mlp = bfr(bfr(sm) + bf2f(d_b2));
      const float y = bfr(bfr(bf2f(d_x) + bfr(sa)) + mlp);
      out[db * DM + n] = (unsigned short)f2bf(y);
    }
    if (wg == 0 && tid == 0) __hip_atomic_store((gu32*)(ws + WS_E), epoch, __ATOMIC_RELAXED, __HIP_MEMORY_SCOPE_AGENT);
  }
}

}

extern "C" void kernel_launch(void* const* d_in, const int* in_sizes, int n_in, void* d_out, int out_size,
                              void* d_ws, size_t ws_size, hipStream_t stream) {
  (void)in_sizes; (void)n_in; (void)out_size; (void)ws_size;
  const unsigned short* x = (const unsigned short*)d_in[0];
  const unsigned short* k = (const unsigned short*)d_in[1];
  const unsigned short* v = (const unsigned short*)d_in[2];
  const unsigned short* wq = (const unsigned short*)d_in[3];
  const unsigned short* wo = (const unsigned short*)d_in[4];
  const unsigned short* w1 = (const unsigned short*)d_in[6];
  const unsigned short* b1 = (const unsigned short*)d_in[7];
  const unsigned short* w2 = (const unsigned short*)d_in[8];
  const unsigned short* b2 = (const unsigned short*)d_in[9];
  hipLaunchKernelGGL(fused_decode_kernel, dim3(NWG), dim3(NTHR), 0, stream, x, k, v, wq, wo, w1, b1, w2, b2,
                     (unsigned short*)d_out, (unsigned char*)d_ws);
}
```

```cpp
#include <hip/hip_runtime.h>
#include <hip/hip_bf16.h>
#include <stdint.h>

namespace {

constexpr int DM = 512, NHKV = 8, SK = 4096, DH = 128, WINDOW = 1024, FF = 1792;
constexpr int NWG = 256, NTHR = 512;
constexpr int NQC = 4096;
constexpr int N_HWG = FF / 16;
constexpr int N_KC = 32 + FF / 128;
constexpr int N_CWG = N_KC * 4;

typedef unsigned v4u __attribute__((ext_vector_type(4)));
typedef unsigned v2u __attribute__((ext_vector_type(2)));
typedef float f32x4 __attribute__((ext_vector_type(4)));
typedef float f32x2 __attribute__((ext_vector_type(2)));
typedef short bf16x8 __attribute__((ext_vector_type(8)));
typedef short s16x4 __attribute__((ext_vector_type(4)));
typedef __attribute__((address_space(1))) unsigned gu32;
typedef __attribute__((address_space(3))) s16x4 lds_s16x4;

constexpr unsigned WS_E = 0;
constexpr unsigned WS_Q = 4096;
constexpr unsigned WS_H = WS_Q + 8 * 1024 * 16;
constexpr unsigned WS_O = WS_H + 8 * 448 * 16;
constexpr unsigned WS_ML = WS_O + 32 * 8 * 4 * 64 * 16;
constexpr unsigned WS_P = WS_ML + 32 * 8 * 4 * 16;
constexpr unsigned WS_END = WS_P + N_KC * 8 * 256 * 16;

#define STAMP(i) do {} while (0)
constexpr unsigned SPIN_CAP = 1u << 18;

constexpr unsigned L_XN = 0;
constexpr unsigned XN_STR = 1040;
constexpr unsigned L_WA = L_XN + 8 * XN_STR;
constexpr unsigned L_VI = L_WA + 2 * 16384;
constexpr unsigned L_WC = L_VI + 65536;
constexpr unsigned L_P = L_WC + 32768;
constexpr unsigned P_STR = 528;
constexpr unsigned L_MX = L_P + 4 * P_STR;
constexpr unsigned L_LS = L_MX + 128;
constexpr unsigned L_ACT = L_LS + 128;
constexpr unsigned ACT_STR = 272;
constexpr unsigned L_PART = L_ACT + 8 * ACT_STR;
constexpr unsigned L_END = L_PART + N_KC * 64;

__device__ __forceinline__ float bf2f(unsigned bits16) { return __builtin_bit_cast(float, bits16 << 16); }
__device__ __forceinline__ float bflo(unsigned w) { return __builtin_bit_cast(float, w << 16); }
__device__ __forceinline__ float bfhi(unsigned w) { return __builtin_bit_cast(float, w & 0xffff0000u); }
__device__ __forceinline__ unsigned f2bf(float f) { return (unsigned)__builtin_bit_cast(unsigned short, (__bf16)f); }
__device__ __forceinline__ float bfr(float f) { return bf2f(f2bf(f)); }
__device__ __forceinline__ unsigned pack2(float lo, float hi) { return f2bf(lo) | (f2bf(hi) << 16); }
template <int CTRL> __device__ __forceinline__ float dpp(float x) {
  return __builtin_bit_cast(float, __builtin_amdgcn_mov_dpp(__builtin_bit_cast(int, x), CTRL, 0xf, 0xf, true));
}
constexpr int XOR1 = 0xB1, ROR1 = 0x121, ROR2 = 0x122, ROR4 = 0x124, ROR8 = 0x128;
__device__ __forceinline__ float row_max(float m) {
  m = fmaxf(m, dpp<ROR1>(m)); m = fmaxf(m, dpp<ROR2>(m)); m = fmaxf(m, dpp<ROR4>(m)); return fmaxf(m, dpp<ROR8>(m));
}
__device__ __forceinline__ float row_sum(float m) {
  m += dpp<ROR1>(m); m += dpp<ROR2>(m); m += dpp<ROR4>(m); return m + dpp<ROR8>(m);
}
__device__ __forceinline__ float wave_sum(float x) {
  x = row_sum(x);
  x += __shfl_xor(x, 16);
  x += __shfl_xor(x, 32);
  return x;
}
__device__ __forceinline__ v4u ldnt(const void* p) { return __builtin_nontemporal_load(reinterpret_cast<const v4u*>(p)); }
__device__ __forceinline__ f32x4 mfma16(v4u a, v4u b, f32x4 c) {
  return __builtin_amdgcn_mfma_f32_16x16x32_bf16(__builtin_bit_cast(bf16x8, a), __builtin_bit_cast(bf16x8, b), c, 0, 0, 0);
}
__device__ __forceinline__ unsigned swz(unsigned row, unsigned ch) {
  return 256u * row + 16u * (ch ^ (((row & 3) << 2) | ((row >> 2) & 3)));
}

__device__ __forceinline__ void put_granule(__amdgpu_buffer_rsrc_t rs, unsigned off, unsigned w0, unsigned w1,
                                            unsigned tagA, unsigned tagB) {
  v4u g = {w0, tagA, w1, tagB};
  __builtin_amdgcn_raw_buffer_store_b128(g, rs, (int)off, 0, 16  );
}
template <int N>
__device__ __forceinline__ void load_granules(__amdgpu_buffer_rsrc_t rs, const unsigned (&off)[N], v4u (&g)[N]) {
#pragma unroll
  for (int n = 0; n < N; ++n) {
    unsigned o = off[n];
    asm volatile("" : "+v"(o)::"memory");
    g[n] = __builtin_amdgcn_raw_buffer_load_b128(rs, (int)o, 0, 16  );
  }
}
template <int N>
__device__ __forceinline__ bool granules_ok(const v4u (&g)[N], unsigned tagA, unsigned tagB) {
  bool ok = true;
#pragma unroll
  for (int n = 0; n < N; ++n) ok = ok && (g[n].y == tagA) && (g[n].w == tagB);
  return __all(ok);
}
template <int N>
__device__ __forceinline__ void get_granules(__amdgpu_buffer_rsrc_t rs, const unsigned (&off)[N], v4u (&g)[N],
                                             unsigned tagA, unsigned tagB) {
  for (unsigned spins = 0;; ++spins) {
    asm volatile("" ::: "memory");
    load_granules<N>(rs, off, g);
    if (granules_ok<N>(g, tagA, tagB) || spins > SPIN_CAP) break;
    __builtin_amdgcn_s_sleep(1);
  }
  asm volatile("" ::: "memory");
}

__global__ void __launch_bounds__(NTHR, 2)
fused_decode_kernel(const unsigned short* __restrict__ x, const unsigned short* __restrict__ kc_,
                    const unsigned short* __restrict__ vc_, const unsigned short* __restrict__ wq,
                    const unsigned short* __restrict__ wo, const unsigned short* __restrict__ w1,
                    const unsigned short* __restrict__ b1, const unsigned short* __restrict__ w2,
                    const unsigned short* __restrict__ b2, unsigned short* __restrict__ out,
                    unsigned char* ws) {
  __shared__ __attribute__((aligned(16))) unsigned char smem[L_END];
  const int tid = threadIdx.x, lane = tid & 63;
  const int wave = __builtin_amdgcn_readfirstlane(tid >> 6);
  const int wg = blockIdx.x;
  const int g4 = lane >> 4, i16 = lane & 15;
  const int tq = i16 >> 2, tp = lane & 3;
  const __amdgpu_buffer_rsrc_t rs = __builtin_amdgcn_make_buffer_rsrc((void*)ws, 0, (int)WS_END, 0x00020000);
  STAMP(0);

  const unsigned epoch = __hip_atomic_load((gu32*)(ws + WS_E), __ATOMIC_RELAXED, __HIP_MEMORY_SCOPE_AGENT) + 1u;
  const v4u xv = *reinterpret_cast<const v4u*>(x + wave * DM + lane * 8);
  v4u wa[4];
  {
    const __amdgpu_buffer_rsrc_t rsW1 =
        __builtin_amdgcn_make_buffer_rsrc((void*)w1, 0, wg < N_HWG ? DM * FF * 2 : 0, 0x00020000);
#pragma unroll
    for (int i = 0; i < 2; ++i) {
      const int pi = tid + NTHR * i, row = pi >> 1, oct = pi & 1;
      wa[i] = ldnt(wq + (size_t)row * NQC + wg * 16 + oct * 8);
      wa[2 + i] = __builtin_amdgcn_raw_buffer_load_b128(rsW1, (row * FF + wg * 16 + oct * 8) * 2, 0, 2);
    }
  }
  const __amdgpu_buffer_rsrc_t rsB1 =
      __builtin_amdgcn_make_buffer_rsrc((void*)b1, 0, wg < N_HWG ? FF * 2 : 0, 0x00020000);
  const v2u b1v = __builtin_amdgcn_raw_buffer_load_b64(rsB1, (wg * 16 + g4 * 4) * 2, 0, 0);
  const int d_n = (wg & 31) * 16 + i16;
  const unsigned d_b2 = b2[d_n];
  const unsigned d_x = x[(wg >> 5) * DM + d_n];
  __builtin_amdgcn_sched_barrier(0);
  __builtin_amdgcn_s_barrier();
  __builtin_amdgcn_sched_barrier(0);

  const int ab = wg >> 5, kvh = (wg >> 2) & 7, sp = wg & 3;
  v4u Kr[8], Vr[8];
  {
    const size_t kv0 = ((size_t)(ab * NHKV + kvh) * SK + (SK - WINDOW) + sp * 256 + wave * 32) * DH;
#pragma unroll
    for (int t = 0; t < 2; ++t)
#pragma unroll
      for (int kk = 0; kk < 4; ++kk) Kr[t * 4 + kk] = ldnt(kc_ + kv0 + (size_t)(16 * t + i16) * DH + 32 * kk + 8 * g4);
#pragma unroll
    for (int i = 0; i < 8; ++i) Vr[i] = ldnt(vc_ + kv0 + (size_t)(4 * i + g4) * DH + i16 * 8);
  }
  __builtin_amdgcn_sched_barrier(0);
  const unsigned tagA = epoch, tagB = epoch ^ 0x9E3779B9u;

  {
    float xf[8];
#pragma unroll
    for (int w = 0; w < 4; ++w) { xf[2 * w] = bflo(xv[w]); xf[2 * w + 1] = bfhi(xv[w]); }
    float s = 0.f;
#pragma unroll
    for (int j = 0; j < 8; ++j) s += xf[j];
    const float mean = wave_sum(s) * (1.0f / DM);
    float q = 0.f;
#pragma unroll
    for (int j = 0; j < 8; ++j) { const float d = xf[j] - mean; q += d * d; }
    const float var = wave_sum(q) * (1.0f / DM);
    const float mb = bfr(mean);
    const float den = bfr(sqrtf(bfr(bfr(var) + 1e-5f)));
    v4u o;
#pragma unroll
    for (int w = 0; w < 4; ++w) o[w] = pack2(bfr(xf[2 * w] - mb) / den, bfr(xf[2 * w + 1] - mb) / den);
    *reinterpret_cast<v4u*>(smem + L_XN + wave * XN_STR + lane * 16) = o;
#pragma unroll
    for (int i = 0; i < 2; ++i) {
      *reinterpret_cast<v4u*>(smem + L_WA + (tid + NTHR * i) * 16) = wa[i];
      *reinterpret_cast<v4u*>(smem + L_WA + 16384 + (tid + NTHR * i) * 16) = wa[2 + i];
    }
  }
  __syncthreads();
  STAMP(1);

  if (wave == 0 || (wave == 1 && wg < N_HWG)) {
    const unsigned img = L_WA + wave * 16384;
    f32x4 acc = {0.f, 0.f, 0.f, 0.f};
#pragma unroll
    for (int ks = 0; ks < 16; ++ks) {
      const s16x4 a0 = __builtin_amdgcn_ds_read_tr16_b64_v4i16((lds_s16x4*)(smem + img + (32 * ks + 8 * g4 + tq) * 32 + tp * 8));
      const s16x4 a1 = __builtin_amdgcn_ds_read_tr16_b64_v4i16((lds_s16x4*)(smem + img + (32 * ks + 8 * g4 + 4 + tq) * 32 + tp * 8));
      const v2u u0 = __builtin_bit_cast(v2u, a0), u1 = __builtin_bit_cast(v2u, a1);
      const v4u af = {u0.x, u0.y, u1.x, u1.y};
      const v4u bfg = *reinterpret_cast<const v4u*>(smem + L_XN + (i16 & 7) * XN_STR + (32 * ks + 8 * g4) * 2);
      acc = mfma16(af, bfg, acc);
    }
    if (i16 < 8) {
      if (wave == 0) {
        put_granule(rs, WS_Q + (unsigned)(i16 * 1024 + wg * 4 + g4) * 16u, pack2(acc[0], acc[1]), pack2(acc[2], acc[3]), tagA, tagB);
      } else {
        float hv[4];
#pragma unroll
        for (int j = 0; j < 4; ++j) {
          const float bj = (j & 1) ? bfhi(b1v[j >> 1]) : bflo(b1v[j >> 1]);
          const float h = bfr(bfr(acc[j]) + bj);
          const float u = 0.7978845608028654f * (h + 0.044715f * h * h * h);
          hv[j] = 0.5f * h * (1.0f + tanhf(u));
        }
        put_granule(rs, WS_H + (unsigned)(i16 * 448 + wg * 4 + g4) * 16u, pack2(hv[0], hv[1]), pack2(hv[2], hv[3]), tagA, tagB);
      }
    }
  }
  STAMP(2);

  const int ckc = wg >> 2, cnc = wg & 3;
  v4u Wc[4];
  {
    asm volatile("" ::"v"(Kr[7]));
    unsigned qoff[8];
    v4u qg[8];
#pragma unroll
    for (int kk = 0; kk < 4; ++kk)
#pragma unroll
      for (int e = 0; e < 2; ++e)
        qoff[kk * 2 + e] = WS_Q + (unsigned)(ab * 1024 + kvh * 128 + (i16 & 3) * 32 + kk * 8 + g4 * 2 + e) * 16u;
    asm volatile("" ::: "memory");
    load_granules<8>(rs, qoff, qg);
    __builtin_amdgcn_sched_barrier(0);
    {
      const unsigned short* wb = (ckc < 32) ? (wo + (size_t)ckc * 128 * DM) : (w2 + (size_t)(ckc - 32) * 128 * DM);
      const __amdgpu_buffer_rsrc_t rsC =
          __builtin_amdgcn_make_buffer_rsrc((void*)wb, 0, wg < N_CWG ? 128 * DM * 2 : 0, 0x00020000);
#pragma unroll
      for (int i = 0; i < 4; ++i)
        Wc[i] = __builtin_amdgcn_raw_buffer_load_b128(rsC, (((tid >> 4) + 32 * i) * DM + cnc * 128 + i16 * 8) * 2, 0, 2);
    }
    __builtin_amdgcn_sched_barrier(0);
    if (!granules_ok<8>(qg, tagA, tagB)) get_granules<8>(rs, qoff, qg, tagA, tagB);
    STAMP(3);
#pragma unroll
    for (int i = 0; i < 8; ++i)
      *reinterpret_cast<v4u*>(smem + L_VI + swz(wave * 32 + 4 * i + g4, i16)) = Vr[i];
    f32x4 sc[2];
#pragma unroll
    for (int t = 0; t < 2; ++t) {
      f32x4 a = {0.f, 0.f, 0.f, 0.f};
#pragma unroll
      for (int kk = 0; kk < 4; ++kk) {
        const v4u qa = {qg[2 * kk].x, qg[2 * kk].z, qg[2 * kk + 1].x, qg[2 * kk + 1].z};
        a = mfma16(qa, Kr[t * 4 + kk], a);
      }
#pragma unroll
      for (int h = 0; h < 4; ++h) sc[t][h] = bfr(bfr(a[h]) * 0.08838834764831845f) * 1.4426950408889634f;
    }
    if (g4 == 0) {
      f32x4 mw;
#pragma unroll
      for (int h = 0; h < 4; ++h) mw[h] = row_max(fmaxf(sc[0][h], sc[1][h]));
      if (i16 == 0) *reinterpret_cast<f32x4*>(smem + L_MX + wave * 16) = mw;
    }
    __syncthreads();
    f32x4 M = *reinterpret_cast<const f32x4*>(smem + L_MX);
#pragma unroll
    for (int w = 1; w < 8; ++w) {
      const f32x4 m2 = *reinterpret_cast<const f32x4*>(smem + L_MX + w * 16);
#pragma unroll
      for (int h = 0; h < 4; ++h) M[h] = fmaxf(M[h], m2[h]);
    }
    if (g4 == 0) {
      f32x4 lw;
#pragma unroll
      for (int h = 0; h < 4; ++h) {
        float l = 0.f;
#pragma unroll
        for (int t = 0; t < 2; ++t) {
          const unsigned pb = f2bf(__builtin_amdgcn_exp2f(sc[t][h] - M[h]));
          *reinterpret_cast<unsigned short*>(smem + L_P + h * P_STR + (wave * 32 + t * 16 + i16) * 2) = (unsigned short)pb;
          l += bf2f(pb);
        }
        lw[h] = row_sum(l);
      }
      if (i16 == 0) *reinterpret_cast<f32x4*>(smem + L_LS + wave * 16) = lw;
    }
    __syncthreads();
    f32x4 o = {0.f, 0.f, 0.f, 0.f};
#pragma unroll
    for (int ks = 0; ks < 8; ++ks) {
      const v4u pa = *reinterpret_cast<const v4u*>(smem + L_P + (i16 & 3) * P_STR + (32 * ks + 8 * g4) * 2);
      const s16x4 b0 = __builtin_amdgcn_ds_read_tr16_b64_v4i16(
          (lds_s16x4*)(smem + L_VI + swz(32 * ks + 8 * g4 + tq, 2 * wave + (tp >> 1)) + 8 * (tp & 1)));
      const s16x4 b1_ = __builtin_amdgcn_ds_read_tr16_b64_v4i16(
          (lds_s16x4*)(smem + L_VI + swz(32 * ks + 8 * g4 + 4 + tq, 2 * wave + (tp >> 1)) + 8 * (tp & 1)));
      const v2u u0 = __builtin_bit_cast(v2u, b0), u1 = __builtin_bit_cast(v2u, b1_);
      o = mfma16(pa, v4u{u0.x, u0.y, u1.x, u1.y}, o);
    }
    const float og = g4 == 0 ? o[0] : g4 == 1 ? o[1] : g4 == 2 ? o[2] : o[3];
    const float on = dpp<XOR1>(og);
    const unsigned slot = (unsigned)(((kvh * 4 + g4) * 8 + ab) * 4 + sp);
    if ((i16 & 1) == 0)
      put_granule(rs, WS_O + (slot * 64u + (unsigned)(wave * 8 + (i16 >> 1))) * 16u, __float_as_uint(og), __float_as_uint(on), tagA, tagB);
    if (wave == 0 && i16 == 0) {
      float Ls = 0.f;
#pragma unroll
      for (int w = 0; w < 8; ++w) Ls += *reinterpret_cast<const float*>(smem + L_LS + w * 16 + g4 * 4);
      const float Mg = g4 == 0 ? M[0] : g4 == 1 ? M[1] : g4 == 2 ? M[2] : M[3];
      put_granule(rs, WS_ML + slot * 16u, __float_as_uint(Mg), __float_as_uint(Ls), tagA, tagB);
    }
  }
  STAMP(4);

  if (wg < N_CWG) {
#pragma unroll
    for (int i = 0; i < 4; ++i) *reinterpret_cast<v4u*>(smem + L_WC + swz((tid >> 4) + 32 * i, i16)) = Wc[i];
    __syncthreads();
    v4u af[4];
#pragma unroll
    for (int ks = 0; ks < 4; ++ks) {
      const s16x4 a0 = __builtin_amdgcn_ds_read_tr16_b64_v4i16(
          (lds_s16x4*)(smem + L_WC + swz(32 * ks + 8 * g4 + tq, 2 * wave + (tp >> 1)) + 8 * (tp & 1)));
      const s16x4 a1 = __builtin_amdgcn_ds_read_tr16_b64_v4i16(
          (lds_s16x4*)(smem + L_WC + swz(32 * ks + 8 * g4 + 4 + tq, 2 * wave + (tp >> 1)) + 8 * (tp & 1)));
      const v2u u0 = __builtin_bit_cast(v2u, a0), u1 = __builtin_bit_cast(v2u, a1);
      af[ks] = v4u{u0.x, u0.y, u1.x, u1.y};
    }
    if (ckc < 32) {
      unsigned off[8];
      v4u g[8];
#pragma unroll
      for (int s = 0; s < 4; ++s) {
        const unsigned slot = (unsigned)((ckc * 8 + wave) * 4 + s);
        off[s] = WS_O + (slot * 64u + lane) * 16u;
        off[4 + s] = WS_ML + slot * 16u;
      }
      get_granules<8>(rs, off, g, tagA, tagB);
      float M = __uint_as_float(g[4].x);
#pragma unroll
      for (int s = 1; s < 4; ++s) M = fmaxf(M, __uint_as_float(g[4 + s].x));
      float Ls = 0.f, o0 = 0.f, o1 = 0.f;
#pragma unroll
      for (int s = 0; s < 4; ++s) {
        const float wt = __builtin_amdgcn_exp2f(__uint_as_float(g[4 + s].x) - M);
        Ls = fmaf(wt, __uint_as_float(g[4 + s].z), Ls);
        o0 = fmaf(wt, __uint_as_float(g[s].x), o0);
        o1 = fmaf(wt, __uint_as_float(g[s].z), o1);
      }
      *reinterpret_cast<unsigned*>(smem + L_ACT + wave * ACT_STR + lane * 4) = pack2(o0 / Ls, o1 / Ls);
      STAMP(5);
    } else if (lane < 32) {
      unsigned off[1] = {WS_H + (unsigned)(wave * 448 + (ckc - 32) * 32 + lane) * 16u};
      v4u g[1];
      get_granules<1>(rs, off, g, tagA, tagB);
      *reinterpret_cast<v2u*>(smem + L_ACT + wave * ACT_STR + lane * 8) = v2u{g[0].x, g[0].z};
    }
    __syncthreads();
    f32x4 acc = {0.f, 0.f, 0.f, 0.f};
#pragma unroll
    for (int ks = 0; ks < 4; ++ks) {
      const v4u bfg = *reinterpret_cast<const v4u*>(smem + L_ACT + (i16 & 7) * ACT_STR + (32 * ks + 8 * g4) * 2);
      acc = mfma16(af[ks], bfg, acc);
    }
    if (i16 < 8) {
      const unsigned po = WS_P + (unsigned)((ckc * 8 + i16) * 256 + cnc * 64 + wave * 8 + g4 * 2) * 16u;
      put_granule(rs, po, __float_as_uint(acc[0]), __float_as_uint(acc[1]), tagA, tagB);
      put_granule(rs, po + 16u, __float_as_uint(acc[2]), __float_as_uint(acc[3]), tagA, tagB);
    }
  }
  STAMP(6);

  {
    const int db = wg >> 5, dg0 = (wg & 31) * 8;
    if (tid < N_KC * 8) {
      const int kc = tid >> 3, gq = tid & 7;
      unsigned off[1] = {WS_P + (unsigned)((kc * 8 + db) * 256 + dg0 + gq) * 16u};
      v4u g[1];
      get_granules<1>(rs, off, g, tagA, tagB);
      *reinterpret_cast<v2u*>(smem + L_PART + kc * 64 + gq * 8) = v2u{g[0].x, g[0].z};
    }
    __syncthreads();
    STAMP(7);
    if (tid < 16) {
      const int n = dg0 * 2 + tid;
      float sa = 0.f, sm = 0.f;
#pragma unroll
      for (int k = 0; k < 32; ++k) sa += *reinterpret_cast<const float*>(smem + L_PART + k * 64 + tid * 4);
#pragma unroll
      for (int k = 32; k < N_KC; ++k) sm += *reinterpret_cast<const float*>(smem + L_PART + k * 64 + tid * 4);
      const float mlp = bfr(bfr(sm) + bf2f(d_b2));
      const float y = bfr(bfr(bf2f(d_x) + bfr(sa)) + mlp);
      out[db * DM + n] = (unsigned short)f2bf(y);
    }
    if (wg == 0 && tid == 0) __hip_atomic_store((gu32*)(ws + WS_E), epoch, __ATOMIC_RELAXED, __HIP_MEMORY_SCOPE_AGENT);
  }
}

}

extern "C" void kernel_launch(void* const* d_in, const int* in_sizes, int n_in, void* d_out, int out_size,
                              void* d_ws, size_t ws_size, hipStream_t stream) {
  (void)in_sizes; (void)n_in; (void)out_size; (void)ws_size;
  const unsigned short* x = (const unsigned short*)d_in[0];
  const unsigned short* k = (const unsigned short*)d_in[1];
  const unsigned short* v = (const unsigned short*)d_in[2];
  const unsigned short* wq = (const unsigned short*)d_in[3];
  const unsigned short* wo = (const unsigned short*)d_in[4];
  const unsigned short* w1 = (const unsigned short*)d_in[6];
  const unsigned short* b1 = (const unsigned short*)d_in[7];
  const unsigned short* w2 = (const unsigned short*)d_in[8];
  const unsigned short* b2 = (const unsigned short*)d_in[9];
  hipLaunchKernelGGL(fused_decode_kernel, dim3(NWG), dim3(NTHR), 0, stream, x, k, v, wq, wo, w1, b1, w2, b2,
                     (unsigned short*)d_out, (unsigned char*)d_ws);
}
```

```cpp
#include <hip/hip_runtime.h>
#include <hip/hip_bf16.h>
#include <stdint.h>

namespace {

constexpr int DM = 512, NHKV = 8, SK = 4096, DH = 128, WINDOW = 1024, FF = 1792;
constexpr int NWG = 256, NTHR = 640;
#define QDELAY 8
constexpr int NQC = 4096;
constexpr int N_HWG = FF / 16;
constexpr int N_KC = 32 + FF / 128;
constexpr int N_CWG = N_KC * 4;

typedef unsigned v4u __attribute__((ext_vector_type(4)));
typedef unsigned v2u __attribute__((ext_vector_type(2)));
typedef float f32x4 __attribute__((ext_vector_type(4)));
typedef float f32x2 __attribute__((ext_vector_type(2)));
typedef short bf16x8 __attribute__((ext_vector_type(8)));
typedef short s16x4 __attribute__((ext_vector_type(4)));
typedef __attribute__((address_space(1))) unsigned gu32;
typedef __attribute__((address_space(3))) s16x4 lds_s16x4;

constexpr unsigned WS_E = 0;
constexpr unsigned WS_Q = 4096;
constexpr unsigned WS_H = WS_Q + 8 * 1024 * 16;
constexpr unsigned WS_O = WS_H + 8 * 448 * 16;
constexpr unsigned WS_ML = WS_O + 32 * 8 * 4 * 64 * 16;
constexpr unsigned WS_P = WS_ML + 32 * 8 * 4 * 16;
constexpr unsigned WS_END = WS_P + N_KC * 8 * 256 * 16;

#define STAMP(i) do {} while (0)
#define STAMP_DECL do {} while (0)
#define STAMP_FLUSH do {} while (0)
constexpr unsigned SPIN_CAP = 1u << 18;

constexpr unsigned L_XN = 0;
constexpr unsigned XN_STR = 1040;
constexpr unsigned L_WA = L_XN + 16 * XN_STR;
constexpr unsigned L_VI = L_WA + 2 * 16384;
constexpr unsigned L_WC = L_VI + 65536;
constexpr unsigned L_P = L_WC + 32768;
constexpr unsigned P_STR = 528;
constexpr unsigned L_MX = L_P + 4 * P_STR;
constexpr unsigned L_LS = L_MX + 128;
constexpr unsigned L_ACT = L_LS + 128;
constexpr unsigned ACT_STR = 272;
constexpr unsigned L_PART = L_ACT + 8 * ACT_STR;
constexpr unsigned L_QS = L_PART + N_KC * 64;
constexpr unsigned L_END = L_QS + 1024;

__device__ __forceinline__ float bf2f(unsigned bits16) { return __builtin_bit_cast(float, bits16 << 16); }
__device__ __forceinline__ float bflo(unsigned w) { return __builtin_bit_cast(float, w << 16); }
__device__ __forceinline__ float bfhi(unsigned w) { return __builtin_bit_cast(float, w & 0xffff0000u); }
__device__ __forceinline__ unsigned f2bf(float f) { return (unsigned)__builtin_bit_cast(unsigned short, (__bf16)f); }
__device__ __forceinline__ float bfr(float f) { return bf2f(f2bf(f)); }
__device__ __forceinline__ unsigned pack2(float lo, float hi) { return f2bf(lo) | (f2bf(hi) << 16); }
template <int CTRL> __device__ __forceinline__ float dpp(float x) {
  return __builtin_bit_cast(float, __builtin_amdgcn_mov_dpp(__builtin_bit_cast(int, x), CTRL, 0xf, 0xf, true));
}
constexpr int XOR1 = 0xB1, ROR1 = 0x121, ROR2 = 0x122, ROR4 = 0x124, ROR8 = 0x128;
__device__ __forceinline__ float row_max(float m) {
  m = fmaxf(m, dpp<ROR1>(m)); m = fmaxf(m, dpp<ROR2>(m)); m = fmaxf(m, dpp<ROR4>(m)); return fmaxf(m, dpp<ROR8>(m));
}
__device__ __forceinline__ float row_sum(float m) {
  m += dpp<ROR1>(m); m += dpp<ROR2>(m); m += dpp<ROR4>(m); return m + dpp<ROR8>(m);
}
__device__ __forceinline__ float wave_sum(float x) {
  x = row_sum(x);
  x += __shfl_xor(x, 16);
  x += __shfl_xor(x, 32);
  return x;
}
__device__ __forceinline__ void wave_lds_sync() {
  __builtin_amdgcn_fence(__ATOMIC_RELEASE, "wavefront");
  __builtin_amdgcn_wave_barrier();
  __builtin_amdgcn_fence(__ATOMIC_ACQUIRE, "wavefront");
}
__device__ __forceinline__ v4u ldnt(const void* p) { return __builtin_nontemporal_load(reinterpret_cast<const v4u*>(p)); }
__device__ __forceinline__ f32x4 mfma16(v4u a, v4u b, f32x4 c) {
  return __builtin_amdgcn_mfma_f32_16x16x32_bf16(__builtin_bit_cast(bf16x8, a), __builtin_bit_cast(bf16x8, b), c, 0, 0, 0);
}
__device__ __forceinline__ unsigned swz(unsigned row, unsigned ch) {
  return 256u * row + 16u * (ch ^ (((row & 3) << 2) | ((row >> 2) & 3)));
}

__device__ __forceinline__ void put_granule(__amdgpu_buffer_rsrc_t rs, unsigned off, unsigned w0, unsigned w1,
                                            unsigned tagA, unsigned tagB) {
  v4u g = {w0, tagA, w1, tagB};
  __builtin_amdgcn_raw_buffer_store_b128(g, rs, (int)off, 0, 16  );
}
template <int N>
__device__ __forceinline__ void load_granules(__amdgpu_buffer_rsrc_t rs, const unsigned (&off)[N], v4u (&g)[N]) {
#pragma unroll
  for (int n = 0; n < N; ++n) {
    unsigned o = off[n];
    asm volatile("" : "+v"(o)::"memory");
    g[n] = __builtin_amdgcn_raw_buffer_load_b128(rs, (int)o, 0, 16  );
  }
}
template <int N>
__device__ __forceinline__ bool granules_ok(const v4u (&g)[N], unsigned tagA, unsigned tagB) {
  bool ok = true;
#pragma unroll
  for (int n = 0; n < N; ++n) ok = ok && (g[n].y == tagA) && (g[n].w == tagB);
  return __all(ok);
}
template <int N>
__device__ __forceinline__ void get_granules(__amdgpu_buffer_rsrc_t rs, const unsigned (&off)[N], v4u (&g)[N],
                                             unsigned tagA, unsigned tagB) {
  for (unsigned spins = 0;; ++spins) {
    asm volatile("" ::: "memory");
    load_granules<N>(rs, off, g);
    if (granules_ok<N>(g, tagA, tagB) || spins > SPIN_CAP) break;
    __builtin_amdgcn_s_sleep(1);
  }
  asm volatile("" ::: "memory");
}

__global__ void __launch_bounds__(NTHR, 3)
fused_decode_kernel(const unsigned short* __restrict__ x, const unsigned short* __restrict__ kc_,
                    const unsigned short* __restrict__ vc_, const unsigned short* __restrict__ wq,
                    const unsigned short* __restrict__ wo, const unsigned short* __restrict__ w1,
                    const unsigned short* __restrict__ b1, const unsigned short* __restrict__ w2,
                    const unsigned short* __restrict__ b2, unsigned short* __restrict__ out,
                    unsigned char* ws) {
  __shared__ __attribute__((aligned(16))) unsigned char smem[L_END];
  const int tid = threadIdx.x, lane = tid & 63;
  const int wave = __builtin_amdgcn_readfirstlane(tid >> 6);
  const int wg = blockIdx.x;
  const int g4 = lane >> 4, i16 = lane & 15;
  const int tq = i16 >> 2, tp = lane & 3;
  const __amdgpu_buffer_rsrc_t rs = __builtin_amdgcn_make_buffer_rsrc((void*)ws, 0, (int)WS_END, 0x00020000);
  const int ab = wg >> 5, kvh = (wg >> 2) & 7, sp = wg & 3;
  STAMP_DECL;
  STAMP(0);
  const unsigned epoch = __hip_atomic_load((gu32*)(ws + WS_E), __ATOMIC_RELAXED, __HIP_MEMORY_SCOPE_AGENT) + 1u;

  if (wave >= 8) {
    const int sv = wave - 8;
    if (sv == 1 && wg >= N_HWG) { __builtin_amdgcn_s_barrier(); __builtin_amdgcn_s_barrier(); return; }
    v4u xv[8], wa[16];
#pragma unroll
    for (int b = 0; b < 8; ++b) xv[b] = *reinterpret_cast<const v4u*>(x + b * DM + lane * 8);
    const unsigned short* wsrc = sv == 0 ? wq : w1;
    const int ldw = sv == 0 ? NQC : FF;
#pragma unroll
    for (int i = 0; i < 16; ++i) {
      const int pi = lane + 64 * i;
      wa[i] = ldnt(wsrc + (size_t)(pi >> 1) * ldw + wg * 16 + (pi & 1) * 8);
    }
    const v2u b1v = *reinterpret_cast<const v2u*>(b1 + (sv == 0 ? 0 : wg * 16 + g4 * 4));
    __builtin_amdgcn_sched_barrier(0);
    __builtin_amdgcn_s_barrier();
    __builtin_amdgcn_sched_barrier(0);
    const unsigned tagA = epoch, tagB = epoch ^ 0x9E3779B9u;
    const unsigned xn = L_XN + sv * (8 * XN_STR), img = L_WA + sv * 16384;
#pragma unroll
    for (int b = 0; b < 8; ++b) {
      float xf[8];
#pragma unroll
      for (int w = 0; w < 4; ++w) { xf[2 * w] = bflo(xv[b][w]); xf[2 * w + 1] = bfhi(xv[b][w]); }
      float sm = 0.f;
#pragma unroll
      for (int j = 0; j < 8; ++j) sm += xf[j];
      const float mean = wave_sum(sm) * (1.0f / DM);
      float q = 0.f;
#pragma unroll
      for (int j = 0; j < 8; ++j) { const float d = xf[j] - mean; q += d * d; }
      const float var = wave_sum(q) * (1.0f / DM);
      const float mb = bfr(mean);
      const float den = bfr(sqrtf(bfr(bfr(var) + 1e-5f)));
      v4u o;
#pragma unroll
      for (int w = 0; w < 4; ++w) o[w] = pack2(bfr(xf[2 * w] - mb) / den, bfr(xf[2 * w + 1] - mb) / den);
      *reinterpret_cast<v4u*>(smem + xn + b * XN_STR + lane * 16) = o;
    }
    STAMP(8);
#pragma unroll
    for (int i = 0; i < 16; ++i) *reinterpret_cast<v4u*>(smem + img + (lane + 64 * i) * 16) = wa[i];
    STAMP(1);
    wave_lds_sync();
    f32x4 acc = {0.f, 0.f, 0.f, 0.f};
#pragma unroll
    for (int ks = 0; ks < 16; ++ks) {
      const s16x4 a0 = __builtin_amdgcn_ds_read_tr16_b64_v4i16((lds_s16x4*)(smem + img + (32 * ks + 8 * g4 + tq) * 32 + tp * 8));
      const s16x4 a1 = __builtin_amdgcn_ds_read_tr16_b64_v4i16((lds_s16x4*)(smem + img + (32 * ks + 8 * g4 + 4 + tq) * 32 + tp * 8));
      const v2u u0 = __builtin_bit_cast(v2u, a0), u1 = __builtin_bit_cast(v2u, a1);
      const v4u bfg = *reinterpret_cast<const v4u*>(smem + xn + (i16 & 7) * XN_STR + (32 * ks + 8 * g4) * 2);
      acc = mfma16(v4u{u0.x, u0.y, u1.x, u1.y}, bfg, acc);
    }
    if (i16 < 8) {
      if (sv == 0) {
        put_granule(rs, WS_Q + (unsigned)(i16 * 1024 + wg * 4 + g4) * 16u, pack2(acc[0], acc[1]), pack2(acc[2], acc[3]), tagA, tagB);
      } else {
        float hv[4];
#pragma unroll
        for (int j = 0; j < 4; ++j) {
          const float bj = (j & 1) ? bfhi(b1v[j >> 1]) : bflo(b1v[j >> 1]);
          const float h = bfr(bfr(acc[j]) + bj);
          const float u = 0.7978845608028654f * (h + 0.044715f * h * h * h);
          hv[j] = 0.5f * h * (1.0f + tanhf(u));
        }
        put_granule(rs, WS_H + (unsigned)(i16 * 448 + wg * 4 + g4) * 16u, pack2(hv[0], hv[1]), pack2(hv[2], hv[3]), tagA, tagB);
      }
    }
    STAMP(2);
    if (sv == 0) {
      __builtin_amdgcn_s_sleep(QDELAY);
      unsigned qoff[2];
      v4u qg[2];
#pragma unroll
      for (int e = 0; e < 2; ++e) qoff[e] = WS_Q + (unsigned)(ab * 1024 + kvh * 128 + lane + 64 * e) * 16u;
      get_granules<2>(rs, qoff, qg, tagA, tagB);
#pragma unroll
      for (int e = 0; e < 2; ++e) *reinterpret_cast<v2u*>(smem + L_QS + (lane + 64 * e) * 8) = v2u{qg[e].x, qg[e].z};
      STAMP(9);
    }
    __syncthreads();
    return;
  }

  const int ckc = wg >> 2, cnc = wg & 3;
  const int d_n = (wg & 31) * 16 + i16;
  const unsigned d_b2 = b2[d_n];
  const unsigned d_x = x[(wg >> 5) * DM + d_n];
  __builtin_amdgcn_sched_barrier(0);
  __builtin_amdgcn_s_barrier();
  __builtin_amdgcn_sched_barrier(0);
  v4u Kr[8], Vr[8], Wc[4];
  {
    const size_t kv0 = ((size_t)(ab * NHKV + kvh) * SK + (SK - WINDOW) + sp * 256 + wave * 32) * DH;
#pragma unroll
    for (int t = 0; t < 2; ++t)
#pragma unroll
      for (int kk = 0; kk < 4; ++kk) Kr[t * 4 + kk] = ldnt(kc_ + kv0 + (size_t)(16 * t + i16) * DH + 32 * kk + 8 * g4);
#pragma unroll
    for (int i = 0; i < 8; ++i) Vr[i] = ldnt(vc_ + kv0 + (size_t)(4 * i + g4) * DH + i16 * 8);
    const unsigned short* wb = (ckc < 32) ? (wo + (size_t)ckc * 128 * DM) : (w2 + (size_t)(ckc - 32) * 128 * DM);
    const __amdgpu_buffer_rsrc_t rsC =
        __builtin_amdgcn_make_buffer_rsrc((void*)wb, 0, wg < N_CWG ? 128 * DM * 2 : 0, 0x00020000);
#pragma unroll
    for (int i = 0; i < 4; ++i)
      Wc[i] = __builtin_amdgcn_raw_buffer_load_b128(rsC, (((tid >> 4) + 32 * i) * DM + cnc * 128 + i16 * 8) * 2, 0, 2);
  }
  __builtin_amdgcn_sched_barrier(0);
  const unsigned tagA = epoch, tagB = epoch ^ 0x9E3779B9u;
  STAMP(1);
  __syncthreads();
  STAMP(2);

  {
    f32x4 sc[2];
#pragma unroll
    for (int t = 0; t < 2; ++t) {
      f32x4 a = {0.f, 0.f, 0.f, 0.f};
#pragma unroll
      for (int kk = 0; kk < 4; ++kk) {
        const v4u qa = *reinterpret_cast<const v4u*>(smem + L_QS + ((i16 & 3) * 128 + 32 * kk + 8 * g4) * 2);
        a = mfma16(qa, Kr[t * 4 + kk], a);
      }
#pragma unroll
      for (int h = 0; h < 4; ++h) sc[t][h] = bfr(bfr(a[h]) * 0.08838834764831845f) * 1.4426950408889634f;
    }
    if (g4 == 0) {
      f32x4 mw;
#pragma unroll
      for (int h = 0; h < 4; ++h) mw[h] = row_max(fmaxf(sc[0][h], sc[1][h]));
      if (i16 == 0) *reinterpret_cast<f32x4*>(smem + L_MX + wave * 16) = mw;
    }
    STAMP(3);
    __syncthreads();
    f32x4 M = *reinterpret_cast<const f32x4*>(smem + L_MX);
#pragma unroll
    for (int w = 1; w < 8; ++w) {
      const f32x4 m2 = *reinterpret_cast<const f32x4*>(smem + L_MX + w * 16);
#pragma unroll
      for (int h = 0; h < 4; ++h) M[h] = fmaxf(M[h], m2[h]);
    }
    if (g4 == 0) {
      f32x4 lw;
#pragma unroll
      for (int h = 0; h < 4; ++h) {
        float l = 0.f;
#pragma unroll
        for (int t = 0; t < 2; ++t) {
          const unsigned pb = f2bf(__builtin_amdgcn_exp2f(sc[t][h] - M[h]));
          *reinterpret_cast<unsigned short*>(smem + L_P + h * P_STR + (wave * 32 + t * 16 + i16) * 2) = (unsigned short)pb;
          l += bf2f(pb);
        }
        lw[h] = row_sum(l);
      }
      if (i16 == 0) *reinterpret_cast<f32x4*>(smem + L_LS + wave * 16) = lw;
    }
#pragma unroll
    for (int i = 0; i < 8; ++i)
      *reinterpret_cast<v4u*>(smem + L_VI + swz(wave * 32 + 4 * i + g4, i16)) = Vr[i];
    STAMP(10);
    __syncthreads();
    f32x4 o = {0.f, 0.f, 0.f, 0.f};
#pragma unroll
    for (int ks = 0; ks < 8; ++ks) {
      const v4u pa = *reinterpret_cast<const v4u*>(smem + L_P + (i16 & 3) * P_STR + (32 * ks + 8 * g4) * 2);
      const s16x4 b0 = __builtin_amdgcn_ds_read_tr16_b64_v4i16(
          (lds_s16x4*)(smem + L_VI + swz(32 * ks + 8 * g4 + tq, 2 * wave + (tp >> 1)) + 8 * (tp & 1)));
      const s16x4 b1_ = __builtin_amdgcn_ds_read_tr16_b64_v4i16(
          (lds_s16x4*)(smem + L_VI + swz(32 * ks + 8 * g4 + 4 + tq, 2 * wave + (tp >> 1)) + 8 * (tp & 1)));
      const v2u u0 = __builtin_bit_cast(v2u, b0), u1 = __builtin_bit_cast(v2u, b1_);
      o = mfma16(pa, v4u{u0.x, u0.y, u1.x, u1.y}, o);
    }
    const float og = g4 == 0 ? o[0] : g4 == 1 ? o[1] : g4 == 2 ? o[2] : o[3];
    const float on = dpp<XOR1>(og);
    const unsigned slot = (unsigned)(((kvh * 4 + g4) * 8 + ab) * 4 + sp);
    if ((i16 & 1) == 0)
      put_granule(rs, WS_O + (slot * 64u + (unsigned)(wave * 8 + (i16 >> 1))) * 16u, __float_as_uint(og), __float_as_uint(on), tagA, tagB);
    if (wave == 0 && i16 == 0) {
      float Ls = 0.f;
#pragma unroll
      for (int w = 0; w < 8; ++w) Ls += *reinterpret_cast<const float*>(smem + L_LS + w * 16 + g4 * 4);
      const float Mg = g4 == 0 ? M[0] : g4 == 1 ? M[1] : g4 == 2 ? M[2] : M[3];
      put_granule(rs, WS_ML + slot * 16u, __float_as_uint(Mg), __float_as_uint(Ls), tagA, tagB);
    }
  }
  STAMP(4);

  if (wg < N_CWG) {
#pragma unroll
    for (int i = 0; i < 4; ++i) *reinterpret_cast<v4u*>(smem + L_WC + swz((tid >> 4) + 32 * i, i16)) = Wc[i];
    __syncthreads();
    v4u af[4];
#pragma unroll
    for (int ks = 0; ks < 4; ++ks) {
      const s16x4 a0 = __builtin_amdgcn_ds_read_tr16_b64_v4i16(
          (lds_s16x4*)(smem + L_WC + swz(32 * ks + 8 * g4 + tq, 2 * wave + (tp >> 1)) + 8 * (tp & 1)));
      const s16x4 a1 = __builtin_amdgcn_ds_read_tr16_b64_v4i16(
          (lds_s16x4*)(smem + L_WC + swz(32 * ks + 8 * g4 + 4 + tq, 2 * wave + (tp >> 1)) + 8 * (tp & 1)));
      const v2u u0 = __builtin_bit_cast(v2u, a0), u1 = __builtin_bit_cast(v2u, a1);
      af[ks] = v4u{u0.x, u0.y, u1.x, u1.y};
    }
    if (ckc < 32) {
      unsigned off[8];
      v4u g[8];
#pragma unroll
      for (int s = 0; s < 4; ++s) {
        const unsigned slot = (unsigned)((ckc * 8 + wave) * 4 + s);
        off[s] = WS_O + (slot * 64u + lane) * 16u;
        off[4 + s] = WS_ML + slot * 16u;
      }
      get_granules<8>(rs, off, g, tagA, tagB);
      float M = __uint_as_float(g[4].x);
#pragma unroll
      for (int s = 1; s < 4; ++s) M = fmaxf(M, __uint_as_float(g[4 + s].x));
      float Ls = 0.f, o0 = 0.f, o1 = 0.f;
#pragma unroll
      for (int s = 0; s < 4; ++s) {
        const float wt = __builtin_amdgcn_exp2f(__uint_as_float(g[4 + s].x) - M);
        Ls = fmaf(wt, __uint_as_float(g[4 + s].z), Ls);
        o0 = fmaf(wt, __uint_as_float(g[s].x), o0);
        o1 = fmaf(wt, __uint_as_float(g[s].z), o1);
      }
      *reinterpret_cast<unsigned*>(smem + L_ACT + wave * ACT_STR + lane * 4) = pack2(o0 / Ls, o1 / Ls);
      STAMP(5);
    } else if (lane < 32) {
      unsigned off[1] = {WS_H + (unsigned)(wave * 448 + (ckc - 32) * 32 + lane) * 16u};
      v4u g[1];
      get_granules<1>(rs, off, g, tagA, tagB);
      *reinterpret_cast<v2u*>(smem + L_ACT + wave * ACT_STR + lane * 8) = v2u{g[0].x, g[0].z};
    }
    __syncthreads();
    f32x4 acc = {0.f, 0.f, 0.f, 0.f};
#pragma unroll
    for (int ks = 0; ks < 4; ++ks) {
      const v4u bfg = *reinterpret_cast<const v4u*>(smem + L_ACT + (i16 & 7) * ACT_STR + (32 * ks + 8 * g4) * 2);
      acc = mfma16(af[ks], bfg, acc);
    }
    if (i16 < 8) {
      const unsigned po = WS_P + (unsigned)((ckc * 8 + i16) * 256 + cnc * 64 + wave * 8 + g4 * 2) * 16u;
      put_granule(rs, po, __float_as_uint(acc[0]), __float_as_uint(acc[1]), tagA, tagB);
      put_granule(rs, po + 16u, __float_as_uint(acc[2]), __float_as_uint(acc[3]), tagA, tagB);
    }
  }
  STAMP(6);

  {
    const int db = wg >> 5, dg0 = (wg & 31) * 8;
    if (tid < N_KC * 8) {
      const int kc = tid >> 3, gq = tid & 7;
      unsigned off[1] = {WS_P + (unsigned)((kc * 8 + db) * 256 + dg0 + gq) * 16u};
      v4u g[1];
      get_granules<1>(rs, off, g, tagA, tagB);
      *reinterpret_cast<v2u*>(smem + L_PART + kc * 64 + gq * 8) = v2u{g[0].x, g[0].z};
    }
    __syncthreads();
    STAMP(7);
    if (tid < 16) {
      const int n = dg0 * 2 + tid;
      float sa = 0.f, sm = 0.f;
#pragma unroll
      for (int k = 0; k < 32; ++k) sa += *reinterpret_cast<const float*>(smem + L_PART + k * 64 + tid * 4);
#pragma unroll
      for (int k = 32; k < N_KC; ++k) sm += *reinterpret_cast<const float*>(smem + L_PART + k * 64 + tid * 4);
      const float mlp = bfr(bfr(sm) + bf2f(d_b2));
      const float y = bfr(bfr(bf2f(d_x) + bfr(sa)) + mlp);
      out[db * DM + n] = (unsigned short)f2bf(y);
    }
    if (wg == 0 && tid == 0) __hip_atomic_store((gu32*)(ws + WS_E), epoch, __ATOMIC_RELAXED, __HIP_MEMORY_SCOPE_AGENT);
  }
  STAMP_FLUSH;
}

}

extern "C" void kernel_launch(void* const* d_in, const int* in_sizes, int n_in, void* d_out, int out_size,
                              void* d_ws, size_t ws_size, hipStream_t stream) {
  (void)in_sizes; (void)n_in; (void)out_size; (void)ws_size;
  const unsigned short* x = (const unsigned short*)d_in[0];
  const unsigned short* k = (const unsigned short*)d_in[1];
  const unsigned short* v = (const unsigned short*)d_in[2];
  const unsigned short* wq = (const unsigned short*)d_in[3];
  const unsigned short* wo = (const unsigned short*)d_in[4];
  const unsigned short* w1 = (const unsigned short*)d_in[6];
  const unsigned short* b1 = (const unsigned short*)d_in[7];
  const unsigned short* w2 = (const unsigned short*)d_in[8];
  const unsigned short* b2 = (const unsigned short*)d_in[9];
  hipLaunchKernelGGL(fused_decode_kernel, dim3(NWG), dim3(NTHR), 0, stream, x, k, v, wq, wo, w1, b1, w2, b2,
                     (unsigned short*)d_out, (unsigned char*)d_ws);
}
```

```cpp
#include <hip/hip_runtime.h>
#include <hip/hip_bf16.h>
#include <stdint.h>

namespace {

constexpr int DM = 512, NHKV = 8, SK = 4096, DH = 128, WINDOW = 1024, FF = 1792;
constexpr int NWG = 256, NTHR = 640;
#define QDELAY 8
constexpr int NQC = 4096;
constexpr int N_HWG = FF / 16;
constexpr int N_KC = 32 + FF / 128;
constexpr int N_CWG = N_KC * 4;

typedef unsigned v4u __attribute__((ext_vector_type(4)));
typedef unsigned v2u __attribute__((ext_vector_type(2)));
typedef float f32x4 __attribute__((ext_vector_type(4)));
typedef float f32x2 __attribute__((ext_vector_type(2)));
typedef short bf16x8 __attribute__((ext_vector_type(8)));
typedef short s16x4 __attribute__((ext_vector_type(4)));
typedef __attribute__((address_space(1))) unsigned gu32;
typedef __attribute__((address_space(3))) s16x4 lds_s16x4;
#define LAS __attribute__((address_space(3)))

constexpr unsigned WS_E = 0;
constexpr unsigned WS_Q = 4096;
constexpr unsigned WS_H = WS_Q + 8 * 1024 * 16;
constexpr unsigned WS_O = WS_H + 8 * 448 * 16;
constexpr unsigned WS_ML = WS_O + 32 * 8 * 4 * 64 * 16;
constexpr unsigned WS_P = WS_ML + 32 * 8 * 4 * 16;
constexpr unsigned WS_END = WS_P + N_KC * 8 * 256 * 16;

#define STAMP(i) do {} while (0)
#define STAMP_DECL do {} while (0)
#define STAMP_FLUSH do {} while (0)
constexpr unsigned SPIN_CAP = 1u << 18;

constexpr unsigned L_XN = 0;
constexpr unsigned XN_STR = 1040;
constexpr unsigned L_WA = L_XN + 8 * XN_STR;
constexpr unsigned L_VI = L_WA + 2 * 16384;
constexpr unsigned L_WC = L_VI + 65536;
constexpr unsigned L_P = L_WC + 32768;
constexpr unsigned P_STR = 528;
constexpr unsigned L_MX = L_P + 4 * P_STR;
constexpr unsigned L_LS = L_MX + 128;
constexpr unsigned L_ACT = L_LS + 128;
constexpr unsigned ACT_STR = 272;
constexpr unsigned L_PART = L_ACT + 8 * ACT_STR;
constexpr unsigned L_QS = L_PART + N_KC * 64;
constexpr unsigned L_CNT = L_QS + 1024;
constexpr unsigned L_END = L_CNT + 16;

__device__ __forceinline__ float bf2f(unsigned bits16) { return __builtin_bit_cast(float, bits16 << 16); }
__device__ __forceinline__ float bflo(unsigned w) { return __builtin_bit_cast(float, w << 16); }
__device__ __forceinline__ float bfhi(unsigned w) { return __builtin_bit_cast(float, w & 0xffff0000u); }
__device__ __forceinline__ unsigned f2bf(float f) { return (unsigned)__builtin_bit_cast(unsigned short, (__bf16)f); }
__device__ __forceinline__ float bfr(float f) { return bf2f(f2bf(f)); }
__device__ __forceinline__ unsigned pack2(float lo, float hi) { return f2bf(lo) | (f2bf(hi) << 16); }
template <int CTRL> __device__ __forceinline__ float dpp(float x) {
  return __builtin_bit_cast(float, __builtin_amdgcn_mov_dpp(__builtin_bit_cast(int, x), CTRL, 0xf, 0xf, true));
}
constexpr int XOR1 = 0xB1, ROR1 = 0x121, ROR2 = 0x122, ROR4 = 0x124, ROR8 = 0x128;
__device__ __forceinline__ float row_max(float m) {
  m = fmaxf(m, dpp<ROR1>(m)); m = fmaxf(m, dpp<ROR2>(m)); m = fmaxf(m, dpp<ROR4>(m)); return fmaxf(m, dpp<ROR8>(m));
}
__device__ __forceinline__ float row_sum(float m) {
  m += dpp<ROR1>(m); m += dpp<ROR2>(m); m += dpp<ROR4>(m); return m + dpp<ROR8>(m);
}
__device__ __forceinline__ float wave_sum(float x) {
  x = row_sum(x);
  auto s = __builtin_amdgcn_permlane16_swap(__float_as_uint(x), __float_as_uint(x), false, false);
  x = __uint_as_float(s[0]) + __uint_as_float(s[1]);
  auto t = __builtin_amdgcn_permlane32_swap(__float_as_uint(x), __float_as_uint(x), false, false);
  return __uint_as_float(t[0]) + __uint_as_float(t[1]);
}
__device__ __forceinline__ void wave_lds_sync() {
  __builtin_amdgcn_fence(__ATOMIC_RELEASE, "wavefront");
  __builtin_amdgcn_wave_barrier();
  __builtin_amdgcn_fence(__ATOMIC_ACQUIRE, "wavefront");
}
__device__ __forceinline__ v4u ldnt(const void* p) { return __builtin_nontemporal_load(reinterpret_cast<const v4u*>(p)); }
__device__ __forceinline__ f32x4 mfma16(v4u a, v4u b, f32x4 c) {
  return __builtin_amdgcn_mfma_f32_16x16x32_bf16(__builtin_bit_cast(bf16x8, a), __builtin_bit_cast(bf16x8, b), c, 0, 0, 0);
}
__device__ __forceinline__ unsigned swz(unsigned row, unsigned ch) {
  return 256u * row + 16u * (ch ^ (((row & 3) << 2) | ((row >> 2) & 3)));
}

__device__ __forceinline__ void put_granule(__amdgpu_buffer_rsrc_t rs, unsigned off, unsigned w0, unsigned w1,
                                            unsigned tagA, unsigned tagB) {
  v4u g = {w0, tagA, w1, tagB};
  __builtin_amdgcn_raw_buffer_store_b128(g, rs, (int)off, 0, 16  );
}
template <int N>
__device__ __forceinline__ void load_granules(__amdgpu_buffer_rsrc_t rs, const unsigned (&off)[N], v4u (&g)[N]) {
#pragma unroll
  for (int n = 0; n < N; ++n) {
    unsigned o = off[n];
    asm volatile("" : "+v"(o)::"memory");
    g[n] = __builtin_amdgcn_raw_buffer_load_b128(rs, (int)o, 0, 16  );
  }
}
template <int N>
__device__ __forceinline__ bool granules_ok(const v4u (&g)[N], unsigned tagA, unsigned tagB) {
  bool ok = true;
#pragma unroll
  for (int n = 0; n < N; ++n) ok = ok && (g[n].y == tagA) && (g[n].w == tagB);
  return __all(ok);
}
template <int N>
__device__ __forceinline__ void get_granules(__amdgpu_buffer_rsrc_t rs, const unsigned (&off)[N], v4u (&g)[N],
                                             unsigned tagA, unsigned tagB) {
  for (unsigned spins = 0;; ++spins) {
    asm volatile("" ::: "memory");
    load_granules<N>(rs, off, g);
    if (granules_ok<N>(g, tagA, tagB) || spins > SPIN_CAP) break;
    __builtin_amdgcn_s_sleep(1);
  }
  asm volatile("" ::: "memory");
}

__global__ void __launch_bounds__(NTHR, 3)
fused_decode_kernel(const unsigned short* __restrict__ x, const unsigned short* __restrict__ kc_,
                    const unsigned short* __restrict__ vc_, const unsigned short* __restrict__ wq,
                    const unsigned short* __restrict__ wo, const unsigned short* __restrict__ w1,
                    const unsigned short* __restrict__ b1, const unsigned short* __restrict__ w2,
                    const unsigned short* __restrict__ b2, unsigned short* __restrict__ out,
                    unsigned char* ws) {
  __shared__ __attribute__((aligned(16))) unsigned char smem[L_END];
  const int tid = threadIdx.x, lane = tid & 63;
  const int wave = __builtin_amdgcn_readfirstlane(tid >> 6);
  const int wg = blockIdx.x;
  const int g4 = lane >> 4, i16 = lane & 15;
  const int tq = i16 >> 2, tp = lane & 3;
  const __amdgpu_buffer_rsrc_t rs = __builtin_amdgcn_make_buffer_rsrc((void*)ws, 0, (int)WS_END, 0x00020000);
  const int ab = wg >> 5, kvh = (wg >> 2) & 7, sp = wg & 3;
  STAMP_DECL;
  STAMP(0);

  if (wave >= 8) {
    __builtin_amdgcn_s_setprio(3);
    const int sv = wave - 8;
    const int cs = sv == 0 ? ((wg & 7) * 32 + (wg >> 3)) : ((wg & 7) * 14 + (wg >> 3));
    const bool has_w = (sv == 0) || ((wg >> 3) < 14);
    v4u xv[4], wa[16];
    unsigned epoch = __hip_atomic_load((gu32*)(ws + WS_E), __ATOMIC_RELAXED, __HIP_MEMORY_SCOPE_AGENT);
#pragma unroll
    for (int b = 0; b < 4; ++b) xv[b] = *reinterpret_cast<const v4u*>(x + (sv * 4 + b) * DM + lane * 8);
    {
      const __amdgpu_buffer_rsrc_t rsW = __builtin_amdgcn_make_buffer_rsrc(
          (void*)(sv == 0 ? wq : w1), 0, has_w ? (sv == 0 ? DM * NQC * 2 : DM * FF * 2) : 0, 0x00020000);
      const int ldw = sv == 0 ? NQC : FF;
#pragma unroll
      for (int i = 0; i < 16; ++i) {
        const int pi = lane + 64 * i;
        wa[i] = __builtin_amdgcn_raw_buffer_load_b128(rsW, ((pi >> 1) * ldw + cs * 16 + (pi & 1) * 8) * 2, 0, 2);
      }
    }
    v2u b1v = *reinterpret_cast<const v2u*>(b1 + ((sv == 0 || !has_w) ? 0 : cs * 16 + g4 * 4));
    __builtin_amdgcn_sched_barrier(0);
    __builtin_amdgcn_s_barrier();
    __builtin_amdgcn_sched_barrier(0);
    const unsigned xn = L_XN, img = L_WA + sv * 16384;
#pragma unroll
    for (int b = 0; b < 4; ++b) {
      f32x2 e[4];
#pragma unroll
      for (int w = 0; w < 4; ++w) e[w] = f32x2{bflo(xv[b][w]), bfhi(xv[b][w])};
      const f32x2 s2 = (e[0] + e[1]) + (e[2] + e[3]);
      const float mean = wave_sum(s2.x + s2.y) * (1.0f / DM);
      f32x2 q2 = {0.f, 0.f};
#pragma unroll
      for (int w = 0; w < 4; ++w) { const f32x2 d = e[w] - mean; q2 += d * d; }
      const float var = wave_sum(q2.x + q2.y) * (1.0f / DM);
      const float mb = bfr(mean);
      const float rinv = __builtin_amdgcn_rcpf(bfr(__builtin_amdgcn_sqrtf(bfr(bfr(var) + 1e-5f))));
      v4u o;
#pragma unroll
      for (int w = 0; w < 4; ++w) {
        const f32x2 t = e[w] - mb;
        const unsigned tb = pack2(t.x, t.y);
        const f32x2 u = f32x2{bflo(tb), bfhi(tb)} * rinv;
        o[w] = pack2(u.x, u.y);
      }
      *reinterpret_cast<v4u*>(smem + xn + (sv * 4 + b) * XN_STR + lane * 16) = o;
    }
    STAMP(8);
    {
      __builtin_amdgcn_fence(__ATOMIC_RELEASE, "workgroup");
      LAS unsigned* cnt = (LAS unsigned*)(smem + L_CNT);
      if (lane == 0) __hip_atomic_fetch_add(cnt, 1u, __ATOMIC_RELAXED, __HIP_MEMORY_SCOPE_WORKGROUP);
      if (!has_w) { __builtin_amdgcn_s_barrier(); return; }
#pragma unroll
      for (int i = 0; i < 16; ++i) *reinterpret_cast<v4u*>(smem + img + (lane + 64 * i) * 16) = wa[i];
      while (__hip_atomic_load(cnt, __ATOMIC_RELAXED, __HIP_MEMORY_SCOPE_WORKGROUP) < 2u) __builtin_amdgcn_s_sleep(1);
      __builtin_amdgcn_fence(__ATOMIC_ACQUIRE, "workgroup");
    }
    asm volatile("" : "+v"(epoch), "+v"(b1v));
    epoch += 1u;
    const unsigned tagA = epoch, tagB = epoch ^ 0x9E3779B9u;
    __builtin_amdgcn_sched_barrier(0);
    STAMP(1);
    __builtin_amdgcn_sched_barrier(0);
    wave_lds_sync();
    v4u afr[16], bfr_[16];
#pragma unroll
    for (int ks = 0; ks < 16; ++ks) {
      const s16x4 a0 = __builtin_amdgcn_ds_read_tr16_b64_v4i16((lds_s16x4*)(smem + img + (32 * ks + 8 * g4 + tq) * 32 + tp * 8));
      const s16x4 a1 = __builtin_amdgcn_ds_read_tr16_b64_v4i16((lds_s16x4*)(smem + img + (32 * ks + 8 * g4 + 4 + tq) * 32 + tp * 8));
      const v2u u0 = __builtin_bit_cast(v2u, a0), u1 = __builtin_bit_cast(v2u, a1);
      afr[ks] = v4u{u0.x, u0.y, u1.x, u1.y};
      bfr_[ks] = *reinterpret_cast<const v4u*>(smem + xn + (i16 & 7) * XN_STR + (32 * ks + 8 * g4) * 2);
    }
    __builtin_amdgcn_sched_barrier(0);
    f32x4 ac4[4] = {{0.f, 0.f, 0.f, 0.f}, {0.f, 0.f, 0.f, 0.f}, {0.f, 0.f, 0.f, 0.f}, {0.f, 0.f, 0.f, 0.f}};
#pragma unroll
    for (int ks = 0; ks < 16; ++ks) ac4[ks & 3] = mfma16(afr[ks], bfr_[ks], ac4[ks & 3]);
    const f32x4 acc = (ac4[0] + ac4[1]) + (ac4[2] + ac4[3]);
    if (i16 < 8) {
      if (sv == 0) {
        put_granule(rs, WS_Q + (unsigned)(i16 * 1024 + cs * 4 + g4) * 16u, pack2(acc[0], acc[1]), pack2(acc[2], acc[3]), tagA, tagB);
      } else {
        float hv[4];
#pragma unroll
        for (int j = 0; j < 4; ++j) {
          const float bj = (j & 1) ? bfhi(b1v[j >> 1]) : bflo(b1v[j >> 1]);
          const float h = bfr(bfr(acc[j]) + bj);
          const float u = 0.7978845608028654f * (h + 0.044715f * h * h * h);
          hv[j] = 0.5f * h * (1.0f + tanhf(u));
        }
        put_granule(rs, WS_H + (unsigned)(i16 * 448 + cs * 4 + g4) * 16u, pack2(hv[0], hv[1]), pack2(hv[2], hv[3]), tagA, tagB);
      }
    }
    STAMP(2);
    if (sv == 0) {
      __builtin_amdgcn_s_sleep(QDELAY);
      unsigned qoff[2];
      v4u qg[2];
#pragma unroll
      for (int e = 0; e < 2; ++e) qoff[e] = WS_Q + (unsigned)(ab * 1024 + kvh * 128 + lane + 64 * e) * 16u;
      get_granules<2>(rs, qoff, qg, tagA, tagB);
#pragma unroll
      for (int e = 0; e < 2; ++e) *reinterpret_cast<v2u*>(smem + L_QS + (lane + 64 * e) * 8) = v2u{qg[e].x, qg[e].z};
      STAMP(9);
    }
    __syncthreads();
    return;
  }

  const int ckc = wg >> 2, cnc = wg & 3;
  const int d_n = (wg & 31) * 16 + i16;
  unsigned epoch = __hip_atomic_load((gu32*)(ws + WS_E), __ATOMIC_RELAXED, __HIP_MEMORY_SCOPE_AGENT);
  const unsigned d_b2 = b2[d_n];
  const unsigned d_x = x[(wg >> 5) * DM + d_n];
  if (tid == 0) __hip_atomic_store((LAS unsigned*)(smem + L_CNT), 0u, __ATOMIC_RELAXED, __HIP_MEMORY_SCOPE_WORKGROUP);
  __builtin_amdgcn_sched_barrier(0);
  asm volatile("s_waitcnt lgkmcnt(0)" ::: "memory");
  __builtin_amdgcn_s_barrier();
  __builtin_amdgcn_sched_barrier(0);
  v4u Kr[8], Vr[8], Wc[4];
  {
    const size_t kv0 = ((size_t)(ab * NHKV + kvh) * SK + (SK - WINDOW) + sp * 256 + wave * 32) * DH;
#pragma unroll
    for (int t = 0; t < 2; ++t)
#pragma unroll
      for (int kk = 0; kk < 4; ++kk) Kr[t * 4 + kk] = ldnt(kc_ + kv0 + (size_t)(16 * t + i16) * DH + 32 * kk + 8 * g4);
#pragma unroll
    for (int i = 0; i < 8; ++i) Vr[i] = ldnt(vc_ + kv0 + (size_t)(4 * i + g4) * DH + i16 * 8);
    const unsigned short* wb = (ckc < 32) ? (wo + (size_t)ckc * 128 * DM) : (w2 + (size_t)(ckc - 32) * 128 * DM);
    const __amdgpu_buffer_rsrc_t rsC =
        __builtin_amdgcn_make_buffer_rsrc((void*)wb, 0, wg < N_CWG ? 128 * DM * 2 : 0, 0x00020000);
#pragma unroll
    for (int i = 0; i < 4; ++i)
      Wc[i] = __builtin_amdgcn_raw_buffer_load_b128(rsC, (((tid >> 4) + 32 * i) * DM + cnc * 128 + i16 * 8) * 2, 0, 2);
  }
  __builtin_amdgcn_sched_barrier(0);
  STAMP(1);
  __syncthreads();
  STAMP(2);
  asm volatile("" : "+v"(epoch));
  epoch += 1u;
  const unsigned tagA = epoch, tagB = epoch ^ 0x9E3779B9u;

  {
    f32x4 sc[2];
#pragma unroll
    for (int t = 0; t < 2; ++t) {
      f32x4 a = {0.f, 0.f, 0.f, 0.f};
#pragma unroll
      for (int kk = 0; kk < 4; ++kk) {
        const v4u qa = *reinterpret_cast<const v4u*>(smem + L_QS + ((i16 & 3) * 128 + 32 * kk + 8 * g4) * 2);
        a = mfma16(qa, Kr[t * 4 + kk], a);
      }
#pragma unroll
      for (int h = 0; h < 4; ++h) sc[t][h] = bfr(bfr(a[h]) * 0.08838834764831845f) * 1.4426950408889634f;
    }
    if (g4 == 0) {
      f32x4 mw;
#pragma unroll
      for (int h = 0; h < 4; ++h) mw[h] = row_max(fmaxf(sc[0][h], sc[1][h]));
      if (i16 == 0) *reinterpret_cast<f32x4*>(smem + L_MX + wave * 16) = mw;
    }
    STAMP(3);
    __syncthreads();
    f32x4 M = *reinterpret_cast<const f32x4*>(smem + L_MX);
#pragma unroll
    for (int w = 1; w < 8; ++w) {
      const f32x4 m2 = *reinterpret_cast<const f32x4*>(smem + L_MX + w * 16);
#pragma unroll
      for (int h = 0; h < 4; ++h) M[h] = fmaxf(M[h], m2[h]);
    }
    if (g4 == 0) {
      f32x4 lw;
#pragma unroll
      for (int h = 0; h < 4; ++h) {
        float l = 0.f;
#pragma unroll
        for (int t = 0; t < 2; ++t) {
          const unsigned pb = f2bf(__builtin_amdgcn_exp2f(sc[t][h] - M[h]));
          *reinterpret_cast<unsigned short*>(smem + L_P + h * P_STR + (wave * 32 + t * 16 + i16) * 2) = (unsigned short)pb;
          l += bf2f(pb);
        }
        lw[h] = row_sum(l);
      }
      if (i16 == 0) *reinterpret_cast<f32x4*>(smem + L_LS + wave * 16) = lw;
    }
#pragma unroll
    for (int i = 0; i < 8; ++i)
      *reinterpret_cast<v4u*>(smem + L_VI + swz(wave * 32 + 4 * i + g4, i16)) = Vr[i];
    STAMP(10);
    __syncthreads();
    f32x4 o = {0.f, 0.f, 0.f, 0.f};
#pragma unroll
    for (int ks = 0; ks < 8; ++ks) {
      const v4u pa = *reinterpret_cast<const v4u*>(smem + L_P + (i16 & 3) * P_STR + (32 * ks + 8 * g4) * 2);
      const s16x4 b0 = __builtin_amdgcn_ds_read_tr16_b64_v4i16(
          (lds_s16x4*)(smem + L_VI + swz(32 * ks + 8 * g4 + tq, 2 * wave + (tp >> 1)) + 8 * (tp & 1)));
      const s16x4 b1_ = __builtin_amdgcn_ds_read_tr16_b64_v4i16(
          (lds_s16x4*)(smem + L_VI + swz(32 * ks + 8 * g4 + 4 + tq, 2 * wave + (tp >> 1)) + 8 * (tp & 1)));
      const v2u u0 = __builtin_bit_cast(v2u, b0), u1 = __builtin_bit_cast(v2u, b1_);
      o = mfma16(pa, v4u{u0.x, u0.y, u1.x, u1.y}, o);
    }
    const float og = g4 == 0 ? o[0] : g4 == 1 ? o[1] : g4 == 2 ? o[2] : o[3];
    const float on = dpp<XOR1>(og);
    const unsigned slot = (unsigned)(((kvh * 4 + g4) * 8 + ab) * 4 + sp);
    if ((i16 & 1) == 0)
      put_granule(rs, WS_O + (slot * 64u + (unsigned)(wave * 8 + (i16 >> 1))) * 16u, __float_as_uint(og), __float_as_uint(on), tagA, tagB);
    if (wave == 0 && i16 == 0) {
      float Ls = 0.f;
#pragma unroll
      for (int w = 0; w < 8; ++w) Ls += *reinterpret_cast<const float*>(smem + L_LS + w * 16 + g4 * 4);
      const float Mg = g4 == 0 ? M[0] : g4 == 1 ? M[1] : g4 == 2 ? M[2] : M[3];
      put_granule(rs, WS_ML + slot * 16u, __float_as_uint(Mg), __float_as_uint(Ls), tagA, tagB);
    }
  }
  STAMP(4);

  if (wg < N_CWG) {
#pragma unroll
    for (int i = 0; i < 4; ++i) *reinterpret_cast<v4u*>(smem + L_WC + swz((tid >> 4) + 32 * i, i16)) = Wc[i];
    __syncthreads();
    v4u af[4];
#pragma unroll
    for (int ks = 0; ks < 4; ++ks) {
      const s16x4 a0 = __builtin_amdgcn_ds_read_tr16_b64_v4i16(
          (lds_s16x4*)(smem + L_WC + swz(32 * ks + 8 * g4 + tq, 2 * wave + (tp >> 1)) + 8 * (tp & 1)));
      const s16x4 a1 = __builtin_amdgcn_ds_read_tr16_b64_v4i16(
          (lds_s16x4*)(smem + L_WC + swz(32 * ks + 8 * g4 + 4 + tq, 2 * wave + (tp >> 1)) + 8 * (tp & 1)));
      const v2u u0 = __builtin_bit_cast(v2u, a0), u1 = __builtin_bit_cast(v2u, a1);
      af[ks] = v4u{u0.x, u0.y, u1.x, u1.y};
    }
    if (ckc < 32) {
      unsigned off[8];
      v4u g[8];
#pragma unroll
      for (int s = 0; s < 4; ++s) {
        const unsigned slot = (unsigned)((ckc * 8 + wave) * 4 + s);
        off[s] = WS_O + (slot * 64u + lane) * 16u;
        off[4 + s] = WS_ML + slot * 16u;
      }
      get_granules<8>(rs, off, g, tagA, tagB);
      float M = __uint_as_float(g[4].x);
#pragma unroll
      for (int s = 1; s < 4; ++s) M = fmaxf(M, __uint_as_float(g[4 + s].x));
      float Ls = 0.f, o0 = 0.f, o1 = 0.f;
#pragma unroll
      for (int s = 0; s < 4; ++s) {
        const float wt = __builtin_amdgcn_exp2f(__uint_as_float(g[4 + s].x) - M);
        Ls = fmaf(wt, __uint_as_float(g[4 + s].z), Ls);
        o0 = fmaf(wt, __uint_as_float(g[s].x), o0);
        o1 = fmaf(wt, __uint_as_float(g[s].z), o1);
      }
      *reinterpret_cast<unsigned*>(smem + L_ACT + wave * ACT_STR + lane * 4) = pack2(o0 / Ls, o1 / Ls);
      STAMP(5);
    } else if (lane < 32) {
      unsigned off[1] = {WS_H + (unsigned)(wave * 448 + (ckc - 32) * 32 + lane) * 16u};
      v4u g[1];
      get_granules<1>(rs, off, g, tagA, tagB);
      *reinterpret_cast<v2u*>(smem + L_ACT + wave * ACT_STR + lane * 8) = v2u{g[0].x, g[0].z};
    }
    __syncthreads();
    f32x4 acc = {0.f, 0.f, 0.f, 0.f};
#pragma unroll
    for (int ks = 0; ks < 4; ++ks) {
      const v4u bfg = *reinterpret_cast<const v4u*>(smem + L_ACT + (i16 & 7) * ACT_STR + (32 * ks + 8 * g4) * 2);
      acc = mfma16(af[ks], bfg, acc);
    }
    if (i16 < 8) {
      const unsigned po = WS_P + (unsigned)((ckc * 8 + i16) * 256 + cnc * 64 + wave * 8 + g4 * 2) * 16u;
      put_granule(rs, po, __float_as_uint(acc[0]), __float_as_uint(acc[1]), tagA, tagB);
      put_granule(rs, po + 16u, __float_as_uint(acc[2]), __float_as_uint(acc[3]), tagA, tagB);
    }
  }
  STAMP(6);

  {
    const int db = wg >> 5, dg0 = (wg & 31) * 8;
    if (tid < N_KC * 8) {
      const int kc = tid >> 3, gq = tid & 7;
      unsigned off[1] = {WS_P + (unsigned)((kc * 8 + db) * 256 + dg0 + gq) * 16u};
      v4u g[1];
      get_granules<1>(rs, off, g, tagA, tagB);
      *reinterpret_cast<v2u*>(smem + L_PART + kc * 64 + gq * 8) = v2u{g[0].x, g[0].z};
    }
    __syncthreads();
    STAMP(7);
    if (tid < 16) {
      const int n = dg0 * 2 + tid;
      float sa = 0.f, sm = 0.f;
#pragma unroll
      for (int k = 0; k < 32; ++k) sa += *reinterpret_cast<const float*>(smem + L_PART + k * 64 + tid * 4);
#pragma unroll
      for (int k = 32; k < N_KC; ++k) sm += *reinterpret_cast<const float*>(smem + L_PART + k * 64 + tid * 4);
      const float mlp = bfr(bfr(sm) + bf2f(d_b2));
      const float y = bfr(bfr(bf2f(d_x) + bfr(sa)) + mlp);
      out[db * DM + n] = (unsigned short)f2bf(y);
    }
    if (wg == 0 && tid == 0) __hip_atomic_store((gu32*)(ws + WS_E), epoch, __ATOMIC_RELAXED, __HIP_MEMORY_SCOPE_AGENT);
  }
  STAMP_FLUSH;
}

}

extern "C" void kernel_launch(void* const* d_in, const int* in_sizes, int n_in, void* d_out, int out_size,
                              void* d_ws, size_t ws_size, hipStream_t stream) {
  (void)in_sizes; (void)n_in; (void)out_size; (void)ws_size;
  const unsigned short* x = (const unsigned short*)d_in[0];
  const unsigned short* k = (const unsigned short*)d_in[1];
  const unsigned short* v = (const unsigned short*)d_in[2];
  const unsigned short* wq = (const unsigned short*)d_in[3];
  const unsigned short* wo = (const unsigned short*)d_in[4];
  const unsigned short* w1 = (const unsigned short*)d_in[6];
  const unsigned short* b1 = (const unsigned short*)d_in[7];
  const unsigned short* w2 = (const unsigned short*)d_in[8];
  const unsigned short* b2 = (const unsigned short*)d_in[9];
  hipLaunchKernelGGL(fused_decode_kernel, dim3(NWG), dim3(NTHR), 0, stream, x, k, v, wq, wo, w1, b1, w2, b2,
                     (unsigned short*)d_out, (unsigned char*)d_ws);
}
```

```cpp
#include <hip/hip_runtime.h>
#include <hip/hip_bf16.h>
#include <stdint.h>

namespace {

constexpr int DM = 512, NHKV = 8, SK = 4096, DH = 128, WINDOW = 1024, FF = 1792;
constexpr int NWG = 256, NTHR = 640;
#define EXP 0
#define WA_AUX 2
#define QDELAY 8
constexpr int NQC = 4096;
constexpr int N_HWG = FF / 16;
constexpr int N_KC = 32 + FF / 128;
constexpr int N_CWG = N_KC * 4;

typedef unsigned v4u __attribute__((ext_vector_type(4)));
typedef unsigned v2u __attribute__((ext_vector_type(2)));
typedef float f32x4 __attribute__((ext_vector_type(4)));
typedef float f32x2 __attribute__((ext_vector_type(2)));
typedef short bf16x8 __attribute__((ext_vector_type(8)));
typedef short s16x4 __attribute__((ext_vector_type(4)));
typedef __attribute__((address_space(1))) unsigned gu32;
typedef __attribute__((address_space(3))) s16x4 lds_s16x4;
#define LAS __attribute__((address_space(3)))

constexpr unsigned WS_E = 0;
constexpr unsigned WS_Q = 4096;
constexpr unsigned WS_H = WS_Q + 8 * 1024 * 16;
constexpr unsigned WS_O = WS_H + 8 * 448 * 16;
constexpr unsigned WS_ML = WS_O + 32 * 8 * 4 * 64 * 16;
constexpr unsigned WS_P = WS_ML + 32 * 8 * 4 * 16;
constexpr unsigned WS_END = WS_P + N_KC * 8 * 256 * 16;

#define STAMP(i) do {} while (0)
#define STAMP_DECL do {} while (0)
#define STAMP_FLUSH do {} while (0)
constexpr unsigned SPIN_CAP = 1u << 18;

constexpr unsigned L_XN = 0;
constexpr unsigned XN_STR = 1040;
constexpr unsigned L_WA = L_XN + 8 * XN_STR;
constexpr unsigned L_VI = L_WA + 2 * 16384;
constexpr unsigned L_WC = L_VI + 65536;
constexpr unsigned L_P = L_WC + 32768;
constexpr unsigned P_STR = 528;
constexpr unsigned L_MX = L_P + 4 * P_STR;
constexpr unsigned L_LS = L_MX + 128;
constexpr unsigned L_ACT = L_LS + 128;
constexpr unsigned ACT_STR = 272;
constexpr unsigned L_PART = L_ACT + 8 * ACT_STR;
constexpr unsigned L_QS = L_PART + N_KC * 64;
constexpr unsigned L_CNT = L_QS + 1024;
constexpr unsigned L_DBG = L_CNT + 16;
constexpr unsigned L_END = L_DBG + 128;

__device__ __forceinline__ float bf2f(unsigned bits16) { return __builtin_bit_cast(float, bits16 << 16); }
__device__ __forceinline__ float bflo(unsigned w) { return __builtin_bit_cast(float, w << 16); }
__device__ __forceinline__ float bfhi(unsigned w) { return __builtin_bit_cast(float, w & 0xffff0000u); }
__device__ __forceinline__ unsigned f2bf(float f) { return (unsigned)__builtin_bit_cast(unsigned short, (__bf16)f); }
__device__ __forceinline__ float bfr(float f) { return bf2f(f2bf(f)); }
__device__ __forceinline__ unsigned pack2(float lo, float hi) { return f2bf(lo) | (f2bf(hi) << 16); }
template <int CTRL> __device__ __forceinline__ float dpp(float x) {
  return __builtin_bit_cast(float, __builtin_amdgcn_mov_dpp(__builtin_bit_cast(int, x), CTRL, 0xf, 0xf, true));
}
constexpr int XOR1 = 0xB1, ROR1 = 0x121, ROR2 = 0x122, ROR4 = 0x124, ROR8 = 0x128;
__device__ __forceinline__ float row_max(float m) {
  m = fmaxf(m, dpp<ROR1>(m)); m = fmaxf(m, dpp<ROR2>(m)); m = fmaxf(m, dpp<ROR4>(m)); return fmaxf(m, dpp<ROR8>(m));
}
__device__ __forceinline__ float row_sum(float m) {
  m += dpp<ROR1>(m); m += dpp<ROR2>(m); m += dpp<ROR4>(m); return m + dpp<ROR8>(m);
}
__device__ __forceinline__ float wave_sum(float x) {
  x = row_sum(x);
  auto s = __builtin_amdgcn_permlane16_swap(__float_as_uint(x), __float_as_uint(x), false, false);
  x = __uint_as_float(s[0]) + __uint_as_float(s[1]);
  auto t = __builtin_amdgcn_permlane32_swap(__float_as_uint(x), __float_as_uint(x), false, false);
  return __uint_as_float(t[0]) + __uint_as_float(t[1]);
}
__device__ __forceinline__ void wave_lds_sync() {
  __builtin_amdgcn_fence(__ATOMIC_RELEASE, "wavefront");
  __builtin_amdgcn_wave_barrier();
  __builtin_amdgcn_fence(__ATOMIC_ACQUIRE, "wavefront");
}
__device__ __forceinline__ v4u ldnt(const void* p) { return __builtin_nontemporal_load(reinterpret_cast<const v4u*>(p)); }
__device__ __forceinline__ f32x4 mfma16(v4u a, v4u b, f32x4 c) {
  return __builtin_amdgcn_mfma_f32_16x16x32_bf16(__builtin_bit_cast(bf16x8, a), __builtin_bit_cast(bf16x8, b), c, 0, 0, 0);
}
__device__ __forceinline__ unsigned swz(unsigned row, unsigned ch) {
  return 256u * row + 16u * (ch ^ (((row & 3) << 2) | ((row >> 2) & 3)));
}

__device__ __forceinline__ void put_granule(__amdgpu_buffer_rsrc_t rs, unsigned off, unsigned w0, unsigned w1,
                                            unsigned tagA, unsigned tagB) {
  v4u g = {w0, tagA, w1, tagB};
  __builtin_amdgcn_raw_buffer_store_b128(g, rs, (int)off, 0, 16  );
}
template <int N>
__device__ __forceinline__ void load_granules(__amdgpu_buffer_rsrc_t rs, const unsigned (&off)[N], v4u (&g)[N]) {
#pragma unroll
  for (int n = 0; n < N; ++n) {
    unsigned o = off[n];
    asm volatile("" : "+v"(o)::"memory");
    g[n] = __builtin_amdgcn_raw_buffer_load_b128(rs, (int)o, 0, 16  );
  }
}
template <int N>
__device__ __forceinline__ bool granules_ok(const v4u (&g)[N], unsigned tagA, unsigned tagB) {
  bool ok = true;
#pragma unroll
  for (int n = 0; n < N; ++n) ok = ok && (g[n].y == tagA) && (g[n].w == tagB);
  return __all(ok);
}
template <int N>
__device__ __forceinline__ void get_granules(__amdgpu_buffer_rsrc_t rs, const unsigned (&off)[N], v4u (&g)[N],
                                             unsigned tagA, unsigned tagB) {
  for (unsigned spins = 0;; ++spins) {
    asm volatile("" ::: "memory");
    load_granules<N>(rs, off, g);
    if (granules_ok<N>(g, tagA, tagB) || spins > SPIN_CAP) break;
    __builtin_amdgcn_s_sleep(1);
  }
  asm volatile("" ::: "memory");
}

__global__ void __launch_bounds__(NTHR, 3)
fused_decode_kernel(const unsigned short* __restrict__ x, const unsigned short* __restrict__ kc_,
                    const unsigned short* __restrict__ vc_, const unsigned short* __restrict__ wq,
                    const unsigned short* __restrict__ wo, const unsigned short* __restrict__ w1,
                    const unsigned short* __restrict__ b1, const unsigned short* __restrict__ w2,
                    const unsigned short* __restrict__ b2, unsigned short* __restrict__ out,
                    unsigned char* ws) {
  __shared__ __attribute__((aligned(16))) unsigned char smem[L_END];
  const int tid = threadIdx.x, lane = tid & 63;
  const int wave = __builtin_amdgcn_readfirstlane(tid >> 6);
  const int wg = blockIdx.x;
  const int g4 = lane >> 4, i16 = lane & 15;
  const int tq = i16 >> 2, tp = lane & 3;
  const __amdgpu_buffer_rsrc_t rs = __builtin_amdgcn_make_buffer_rsrc((void*)ws, 0, (int)WS_END, 0x00020000);
  const int ab = wg >> 5, kvh = (wg >> 2) & 7, sp = wg & 3;
  STAMP_DECL;
  STAMP(0);

  if (EXP == 4 && wave >= 8) return;
  if (EXP == 7 && wave >= 8) { __builtin_amdgcn_s_barrier(); __builtin_amdgcn_s_barrier(); return; }
  if (wave >= 8) {
    __builtin_amdgcn_s_setprio(3);
    const int sv = wave - 8;
    const int cs = sv == 0 ? ((wg & 7) * 32 + (wg >> 3)) : ((wg & 7) * 14 + (wg >> 3));
    const bool has_w = (sv == 0) || ((wg >> 3) < 14);
    v4u xv[4], wa[16];
    unsigned epoch = __hip_atomic_load((gu32*)(ws + WS_E), __ATOMIC_RELAXED, __HIP_MEMORY_SCOPE_AGENT);
#pragma unroll
    for (int b = 0; b < 4; ++b) xv[b] = *reinterpret_cast<const v4u*>(x + (sv * 4 + b) * DM + lane * 8);
    {
      const __amdgpu_buffer_rsrc_t rsW = __builtin_amdgcn_make_buffer_rsrc(
          (void*)(sv == 0 ? wq : w1), 0, (has_w && EXP != 2) ? (sv == 0 ? DM * NQC * 2 : DM * FF * 2) : 0, 0x00020000);
      const int ldw = sv == 0 ? NQC : FF;
#pragma unroll
      for (int i = 0; i < 16; ++i) {
        const int pi = lane + 64 * i;
        wa[i] = __builtin_amdgcn_raw_buffer_load_b128(rsW, ((pi >> 1) * ldw + cs * 16 + (pi & 1) * 8) * 2, 0, WA_AUX);
      }
    }
    v2u b1v = *reinterpret_cast<const v2u*>(b1 + ((sv == 0 || !has_w) ? 0 : cs * 16 + g4 * 4));
    __builtin_amdgcn_sched_barrier(0);
    __builtin_amdgcn_s_barrier();
    __builtin_amdgcn_sched_barrier(0);
    const unsigned xn = L_XN, img = L_WA + sv * 16384;
#pragma unroll
    for (int b = 0; b < 4; ++b) {
      f32x2 e[4];
#pragma unroll
      for (int w = 0; w < 4; ++w) e[w] = f32x2{bflo(xv[b][w]), bfhi(xv[b][w])};
      const f32x2 s2 = (e[0] + e[1]) + (e[2] + e[3]);
      const float mean = wave_sum(s2.x + s2.y) * (1.0f / DM);
      f32x2 q2 = {0.f, 0.f};
#pragma unroll
      for (int w = 0; w < 4; ++w) { const f32x2 d = e[w] - mean; q2 += d * d; }
      const float var = wave_sum(q2.x + q2.y) * (1.0f / DM);
      const float mb = bfr(mean);
      const float rinv = __builtin_amdgcn_rcpf(bfr(__builtin_amdgcn_sqrtf(bfr(bfr(var) + 1e-5f))));
      v4u o;
#pragma unroll
      for (int w = 0; w < 4; ++w) {
        const f32x2 t = e[w] - mb;
        const unsigned tb = pack2(t.x, t.y);
        const f32x2 u = f32x2{bflo(tb), bfhi(tb)} * rinv;
        o[w] = pack2(u.x, u.y);
      }
      *reinterpret_cast<v4u*>(smem + xn + (sv * 4 + b) * XN_STR + lane * 16) = o;
    }
    STAMP(8);
    {
      __builtin_amdgcn_fence(__ATOMIC_RELEASE, "workgroup");
      LAS unsigned* cnt = (LAS unsigned*)(smem + L_CNT);
      if (lane == 0) __hip_atomic_fetch_add(cnt, 1u, __ATOMIC_RELAXED, __HIP_MEMORY_SCOPE_WORKGROUP);
      if (!has_w) { __builtin_amdgcn_s_barrier(); return; }
#pragma unroll
      for (int i = 0; i < 16; ++i) *reinterpret_cast<v4u*>(smem + img + (lane + 64 * i) * 16) = wa[i];
      while (__hip_atomic_load(cnt, __ATOMIC_RELAXED, __HIP_MEMORY_SCOPE_WORKGROUP) < 2u) __builtin_amdgcn_s_sleep(1);
      __builtin_amdgcn_fence(__ATOMIC_ACQUIRE, "workgroup");
    }
    asm volatile("" : "+v"(epoch), "+v"(b1v));
    epoch += 1u;
    const unsigned tagA = epoch, tagB = epoch ^ 0x9E3779B9u;
    __builtin_amdgcn_sched_barrier(0);
    STAMP(1);
    __builtin_amdgcn_sched_barrier(0);
    wave_lds_sync();
    v4u afr[16], bfr_[16];
#pragma unroll
    for (int ks = 0; ks < 16; ++ks) {
      const s16x4 a0 = __builtin_amdgcn_ds_read_tr16_b64_v4i16((lds_s16x4*)(smem + img + (32 * ks + 8 * g4 + tq) * 32 + tp * 8));
      const s16x4 a1 = __builtin_amdgcn_ds_read_tr16_b64_v4i16((lds_s16x4*)(smem + img + (32 * ks + 8 * g4 + 4 + tq) * 32 + tp * 8));
      const v2u u0 = __builtin_bit_cast(v2u, a0), u1 = __builtin_bit_cast(v2u, a1);
      afr[ks] = v4u{u0.x, u0.y, u1.x, u1.y};
      bfr_[ks] = *reinterpret_cast<const v4u*>(smem + xn + (i16 & 7) * XN_STR + (32 * ks + 8 * g4) * 2);
    }
    __builtin_amdgcn_sched_barrier(0);
    f32x4 ac4[4] = {{0.f, 0.f, 0.f, 0.f}, {0.f, 0.f, 0.f, 0.f}, {0.f, 0.f, 0.f, 0.f}, {0.f, 0.f, 0.f, 0.f}};
#pragma unroll
    for (int ks = 0; ks < 16; ++ks) ac4[ks & 3] = mfma16(afr[ks], bfr_[ks], ac4[ks & 3]);
    const f32x4 acc = (ac4[0] + ac4[1]) + (ac4[2] + ac4[3]);
    if (i16 < 8) {
      if (sv == 0) {
        put_granule(rs, WS_Q + (unsigned)(i16 * 1024 + cs * 4 + g4) * 16u, pack2(acc[0], acc[1]), pack2(acc[2], acc[3]), tagA, tagB);
      } else {
        float hv[4];
#pragma unroll
        for (int j = 0; j < 4; ++j) {
          const float bj = (j & 1) ? bfhi(b1v[j >> 1]) : bflo(b1v[j >> 1]);
          const float h = bfr(bfr(acc[j]) + bj);
          const float u = 0.7978845608028654f * (h + 0.044715f * h * h * h);
          hv[j] = 0.5f * h * (1.0f + tanhf(u));
        }
        put_granule(rs, WS_H + (unsigned)(i16 * 448 + cs * 4 + g4) * 16u, pack2(hv[0], hv[1]), pack2(hv[2], hv[3]), tagA, tagB);
      }
    }
    STAMP(2);
    if (sv == 0) {
      __builtin_amdgcn_s_sleep(QDELAY);
      unsigned qoff[2];
      v4u qg[2];
#pragma unroll
      for (int e = 0; e < 2; ++e) qoff[e] = WS_Q + (unsigned)(ab * 1024 + kvh * 128 + lane + 64 * e) * 16u;
      get_granules<2>(rs, qoff, qg, tagA, tagB);
#pragma unroll
      for (int e = 0; e < 2; ++e) *reinterpret_cast<v2u*>(smem + L_QS + (lane + 64 * e) * 8) = v2u{qg[e].x, qg[e].z};
      STAMP(9);
    }
    __syncthreads();
    return;
  }

  const int ckc = wg >> 2, cnc = wg & 3;
  const int d_n = (wg & 31) * 16 + i16;
  unsigned epoch = __hip_atomic_load((gu32*)(ws + WS_E), __ATOMIC_RELAXED, __HIP_MEMORY_SCOPE_AGENT);
  const unsigned d_b2 = b2[d_n];
  const unsigned d_x = x[(wg >> 5) * DM + d_n];
  if (tid == 0) __hip_atomic_store((LAS unsigned*)(smem + L_CNT), 0u, __ATOMIC_RELAXED, __HIP_MEMORY_SCOPE_WORKGROUP);
  __builtin_amdgcn_sched_barrier(0);
  asm volatile("s_waitcnt lgkmcnt(0)" ::: "memory");
  __builtin_amdgcn_s_barrier();
  __builtin_amdgcn_sched_barrier(0);
  v4u Kr[8], Vr[8], Wc[4];
  {
    const size_t kv0 = ((size_t)(ab * NHKV + kvh) * SK + (SK - WINDOW) + sp * 256 + wave * 32) * DH;
#pragma unroll
    for (int t = 0; t < 2; ++t)
#pragma unroll
      for (int kk = 0; kk < 4; ++kk) Kr[t * 4 + kk] = ldnt(kc_ + kv0 + (size_t)(16 * t + i16) * DH + 32 * kk + 8 * g4);
#pragma unroll
    for (int i = 0; i < 8; ++i) Vr[i] = ldnt(vc_ + kv0 + (size_t)(4 * i + g4) * DH + i16 * 8);
    const unsigned short* wb = (ckc < 32) ? (wo + (size_t)ckc * 128 * DM) : (w2 + (size_t)(ckc - 32) * 128 * DM);
    const __amdgpu_buffer_rsrc_t rsC =
        __builtin_amdgcn_make_buffer_rsrc((void*)wb, 0, (wg < N_CWG && EXP != 3) ? 128 * DM * 2 : 0, 0x00020000);
#pragma unroll
    for (int i = 0; i < 4; ++i)
      Wc[i] = __builtin_amdgcn_raw_buffer_load_b128(rsC, (((tid >> 4) + 32 * i) * DM + cnc * 128 + i16 * 8) * 2, 0, 2);
  }
  __builtin_amdgcn_sched_barrier(0);
  STAMP(1);
  __syncthreads();
  STAMP(2);
  asm volatile("" : "+v"(epoch));
  epoch += 1u;
  const unsigned tagA = epoch, tagB = epoch ^ 0x9E3779B9u;

  {
    f32x4 sc[2];
#pragma unroll
    for (int t = 0; t < 2; ++t) {
      f32x4 a = {0.f, 0.f, 0.f, 0.f};
#pragma unroll
      for (int kk = 0; kk < 4; ++kk) {
        const v4u qa = *reinterpret_cast<const v4u*>(smem + L_QS + ((i16 & 3) * 128 + 32 * kk + 8 * g4) * 2);
        a = mfma16(qa, Kr[t * 4 + kk], a);
      }
#pragma unroll
      for (int h = 0; h < 4; ++h) sc[t][h] = bfr(bfr(a[h]) * 0.08838834764831845f) * 1.4426950408889634f;
    }
    if (g4 == 0) {
      f32x4 mw;
#pragma unroll
      for (int h = 0; h < 4; ++h) mw[h] = row_max(fmaxf(sc[0][h], sc[1][h]));
      if (i16 == 0) *reinterpret_cast<f32x4*>(smem + L_MX + wave * 16) = mw;
    }
    STAMP(3);
    __syncthreads();
    __builtin_amdgcn_sched_barrier(0);
    STAMP(8);
    __builtin_amdgcn_sched_barrier(0);
    f32x4 M = *reinterpret_cast<const f32x4*>(smem + L_MX);
#pragma unroll
    for (int w = 1; w < 8; ++w) {
      const f32x4 m2 = *reinterpret_cast<const f32x4*>(smem + L_MX + w * 16);
#pragma unroll
      for (int h = 0; h < 4; ++h) M[h] = fmaxf(M[h], m2[h]);
    }
    if (g4 == 0) {
      f32x4 lw;
#pragma unroll
      for (int h = 0; h < 4; ++h) {
        float l = 0.f;
#pragma unroll
        for (int t = 0; t < 2; ++t) {
          const unsigned pb = f2bf(__builtin_amdgcn_exp2f(sc[t][h] - M[h]));
          *reinterpret_cast<unsigned short*>(smem + L_P + h * P_STR + (wave * 32 + t * 16 + i16) * 2) = (unsigned short)pb;
          l += bf2f(pb);
        }
        lw[h] = row_sum(l);
      }
      if (i16 == 0) *reinterpret_cast<f32x4*>(smem + L_LS + wave * 16) = lw;
    }
    __builtin_amdgcn_sched_barrier(0);
    STAMP(9);
    __builtin_amdgcn_sched_barrier(0);
#pragma unroll
    for (int i = 0; i < 8; ++i)
      *reinterpret_cast<v4u*>(smem + L_VI + swz(wave * 32 + 4 * i + g4, i16)) = Vr[i];
    STAMP(10);
    __syncthreads();
    v4u pa[8], vb[8];
#pragma unroll
    for (int ks = 0; ks < 8; ++ks) {
      pa[ks] = *reinterpret_cast<const v4u*>(smem + L_P + (i16 & 3) * P_STR + (32 * ks + 8 * g4) * 2);
      const s16x4 b0 = __builtin_amdgcn_ds_read_tr16_b64_v4i16(
          (lds_s16x4*)(smem + L_VI + swz(32 * ks + 8 * g4 + tq, 2 * wave + (tp >> 1)) + 8 * (tp & 1)));
      const s16x4 b1_ = __builtin_amdgcn_ds_read_tr16_b64_v4i16(
          (lds_s16x4*)(smem + L_VI + swz(32 * ks + 8 * g4 + 4 + tq, 2 * wave + (tp >> 1)) + 8 * (tp & 1)));
      const v2u u0 = __builtin_bit_cast(v2u, b0), u1 = __builtin_bit_cast(v2u, b1_);
      vb[ks] = v4u{u0.x, u0.y, u1.x, u1.y};
    }
    __builtin_amdgcn_sched_barrier(0);
    f32x4 o2[2] = {{0.f, 0.f, 0.f, 0.f}, {0.f, 0.f, 0.f, 0.f}};
#pragma unroll
    for (int ks = 0; ks < 8; ++ks) o2[ks & 1] = mfma16(pa[ks], vb[ks], o2[ks & 1]);
    const f32x4 o = o2[0] + o2[1];
    const float og = g4 == 0 ? o[0] : g4 == 1 ? o[1] : g4 == 2 ? o[2] : o[3];
    const float on = dpp<XOR1>(og);
    const unsigned slot = (unsigned)(((kvh * 4 + g4) * 8 + ab) * 4 + sp);
    if ((i16 & 1) == 0)
      put_granule(rs, WS_O + (slot * 64u + (unsigned)(wave * 8 + (i16 >> 1))) * 16u, __float_as_uint(og), __float_as_uint(on), tagA, tagB);
    if (wave == 0 && i16 == 0) {
      float Ls = 0.f;
#pragma unroll
      for (int w = 0; w < 8; ++w) Ls += *reinterpret_cast<const float*>(smem + L_LS + w * 16 + g4 * 4);
      const float Mg = g4 == 0 ? M[0] : g4 == 1 ? M[1] : g4 == 2 ? M[2] : M[3];
      put_granule(rs, WS_ML + slot * 16u, __float_as_uint(Mg), __float_as_uint(Ls), tagA, tagB);
    }
  }
  STAMP(4);

  if (wg < N_CWG) {
#pragma unroll
    for (int i = 0; i < 4; ++i) *reinterpret_cast<v4u*>(smem + L_WC + swz((tid >> 4) + 32 * i, i16)) = Wc[i];
    __syncthreads();
    v4u af[4];
#pragma unroll
    for (int ks = 0; ks < 4; ++ks) {
      const s16x4 a0 = __builtin_amdgcn_ds_read_tr16_b64_v4i16(
          (lds_s16x4*)(smem + L_WC + swz(32 * ks + 8 * g4 + tq, 2 * wave + (tp >> 1)) + 8 * (tp & 1)));
      const s16x4 a1 = __builtin_amdgcn_ds_read_tr16_b64_v4i16(
          (lds_s16x4*)(smem + L_WC + swz(32 * ks + 8 * g4 + 4 + tq, 2 * wave + (tp >> 1)) + 8 * (tp & 1)));
      const v2u u0 = __builtin_bit_cast(v2u, a0), u1 = __builtin_bit_cast(v2u, a1);
      af[ks] = v4u{u0.x, u0.y, u1.x, u1.y};
    }
    if (ckc < 32) {
      unsigned off[8];
      v4u g[8];
#pragma unroll
      for (int s = 0; s < 4; ++s) {
        const unsigned slot = (unsigned)((ckc * 8 + wave) * 4 + s);
        off[s] = WS_O + (slot * 64u + lane) * 16u;
        off[4 + s] = WS_ML + slot * 16u;
      }
      get_granules<8>(rs, off, g, tagA, tagB);
      float M = __uint_as_float(g[4].x);
#pragma unroll
      for (int s = 1; s < 4; ++s) M = fmaxf(M, __uint_as_float(g[4 + s].x));
      float Ls = 0.f, o0 = 0.f, o1 = 0.f;
#pragma unroll
      for (int s = 0; s < 4; ++s) {
        const float wt = __builtin_amdgcn_exp2f(__uint_as_float(g[4 + s].x) - M);
        Ls = fmaf(wt, __uint_as_float(g[4 + s].z), Ls);
        o0 = fmaf(wt, __uint_as_float(g[s].x), o0);
        o1 = fmaf(wt, __uint_as_float(g[s].z), o1);
      }
      *reinterpret_cast<unsigned*>(smem + L_ACT + wave * ACT_STR + lane * 4) = pack2(o0 / Ls, o1 / Ls);
      STAMP(5);
    } else if (lane < 32) {
      unsigned off[1] = {WS_H + (unsigned)(wave * 448 + (ckc - 32) * 32 + lane) * 16u};
      v4u g[1];
      get_granules<1>(rs, off, g, tagA, tagB);
      *reinterpret_cast<v2u*>(smem + L_ACT + wave * ACT_STR + lane * 8) = v2u{g[0].x, g[0].z};
    }
    __syncthreads();
    f32x4 acc = {0.f, 0.f, 0.f, 0.f};
#pragma unroll
    for (int ks = 0; ks < 4; ++ks) {
      const v4u bfg = *reinterpret_cast<const v4u*>(smem + L_ACT + (i16 & 7) * ACT_STR + (32 * ks + 8 * g4) * 2);
      acc = mfma16(af[ks], bfg, acc);
    }
    if (i16 < 8) {
      const unsigned po = WS_P + (unsigned)((ckc * 8 + i16) * 256 + cnc * 64 + wave * 8 + g4 * 2) * 16u;
      put_granule(rs, po, __float_as_uint(acc[0]), __float_as_uint(acc[1]), tagA, tagB);
      put_granule(rs, po + 16u, __float_as_uint(acc[2]), __float_as_uint(acc[3]), tagA, tagB);
    }
  }
  STAMP(6);

  {
    const int db = wg >> 5, dg0 = (wg & 31) * 8;
    if (tid < N_KC * 8) {
      const int kc = tid >> 3, gq = tid & 7;
      unsigned off[1] = {WS_P + (unsigned)((kc * 8 + db) * 256 + dg0 + gq) * 16u};
      v4u g[1];
      get_granules<1>(rs, off, g, tagA, tagB);
      *reinterpret_cast<v2u*>(smem + L_PART + kc * 64 + gq * 8) = v2u{g[0].x, g[0].z};
    }
    __syncthreads();
    STAMP(7);
    if (tid < 16) {
      const int n = dg0 * 2 + tid;
      float sa = 0.f, sm = 0.f;
#pragma unroll
      for (int k = 0; k < 32; ++k) sa += *reinterpret_cast<const float*>(smem + L_PART + k * 64 + tid * 4);
#pragma unroll
      for (int k = 32; k < N_KC; ++k) sm += *reinterpret_cast<const float*>(smem + L_PART + k * 64 + tid * 4);
      const float mlp = bfr(bfr(sm) + bf2f(d_b2));
      const float y = bfr(bfr(bf2f(d_x) + bfr(sa)) + mlp);
      out[db * DM + n] = (unsigned short)f2bf(y);
    }
    if (wg == 0 && tid == 0) __hip_atomic_store((gu32*)(ws + WS_E), epoch, __ATOMIC_RELAXED, __HIP_MEMORY_SCOPE_AGENT);
  }
  STAMP_FLUSH;
}

}

extern "C" void kernel_launch(void* const* d_in, const int* in_sizes, int n_in, void* d_out, int out_size,
                              void* d_ws, size_t ws_size, hipStream_t stream) {
  (void)in_sizes; (void)n_in; (void)out_size; (void)ws_size;
  const unsigned short* x = (const unsigned short*)d_in[0];
  const unsigned short* k = (const unsigned short*)d_in[1];
  const unsigned short* v = (const unsigned short*)d_in[2];
  const unsigned short* wq = (const unsigned short*)d_in[3];
  const unsigned short* wo = (const unsigned short*)d_in[4];
  const unsigned short* w1 = (const unsigned short*)d_in[6];
  const unsigned short* b1 = (const unsigned short*)d_in[7];
  const unsigned short* w2 = (const unsigned short*)d_in[8];
  const unsigned short* b2 = (const unsigned short*)d_in[9];
  hipLaunchKernelGGL(fused_decode_kernel, dim3(NWG), dim3(NTHR), 0, stream, x, k, v, wq, wo, w1, b1, w2, b2,
                     (unsigned short*)d_out, (unsigned char*)d_ws);
}
```

```cpp
#include <hip/hip_runtime.h>
#include <hip/hip_bf16.h>
#include <stdint.h>

namespace {

constexpr int DM = 512, NHKV = 8, SK = 4096, DH = 128, WINDOW = 1024, FF = 1792;
constexpr int NWG = 256, NTHR = 640;
#define EXP 0
#define WA_AUX 2
#define QDELAY 8
constexpr int NQC = 4096;
constexpr int N_HWG = FF / 16;
constexpr int N_KC = 32 + FF / 128;
constexpr int N_CWG = N_KC * 4;

typedef unsigned v4u __attribute__((ext_vector_type(4)));
typedef unsigned v2u __attribute__((ext_vector_type(2)));
typedef float f32x4 __attribute__((ext_vector_type(4)));
typedef float f32x2 __attribute__((ext_vector_type(2)));
typedef short bf16x8 __attribute__((ext_vector_type(8)));
typedef short s16x4 __attribute__((ext_vector_type(4)));
typedef __attribute__((address_space(1))) unsigned gu32;
typedef __attribute__((address_space(3))) s16x4 lds_s16x4;
#define LAS __attribute__((address_space(3)))

constexpr unsigned WS_E = 0;
constexpr unsigned WS_Q = 4096;
constexpr unsigned WS_H = WS_Q + 8 * 1024 * 16;
constexpr unsigned WS_O = WS_H + 8 * 448 * 16;
constexpr unsigned WS_ML = WS_O + 32 * 8 * 4 * 64 * 16;
constexpr unsigned WS_P = WS_ML + 32 * 8 * 4 * 16;
constexpr unsigned WS_END = WS_P + N_KC * 8 * 256 * 16;

#define STAMP(i) do {} while (0)
#define STAMP_DECL do {} while (0)
#define STAMP_FLUSH do {} while (0)
constexpr unsigned SPIN_CAP = 1u << 18;

constexpr unsigned L_XN = 0;
constexpr unsigned XN_STR = 1040;
constexpr unsigned L_WA = L_XN + 8 * XN_STR;
constexpr unsigned L_VI = L_WA + 2 * 16384;
constexpr unsigned L_WC = L_VI + 65536;
constexpr unsigned L_P = L_WC + 32768;
constexpr unsigned P_STR = 528;
constexpr unsigned L_MX = L_P + 4 * P_STR;
constexpr unsigned L_LS = L_MX + 128;
constexpr unsigned L_ACT = L_LS + 128;
constexpr unsigned ACT_STR = 272;
constexpr unsigned L_PART = L_ACT + 8 * ACT_STR;
constexpr unsigned L_QS = L_PART + N_KC * 64;
constexpr unsigned L_CNT = L_QS + 1024;
constexpr unsigned L_DBG = L_CNT + 16;
constexpr unsigned L_END = L_DBG + 128;

__device__ __forceinline__ float bf2f(unsigned bits16) { return __builtin_bit_cast(float, bits16 << 16); }
__device__ __forceinline__ float bflo(unsigned w) { return __builtin_bit_cast(float, w << 16); }
__device__ __forceinline__ float bfhi(unsigned w) { return __builtin_bit_cast(float, w & 0xffff0000u); }
__device__ __forceinline__ unsigned f2bf(float f) { return (unsigned)__builtin_bit_cast(unsigned short, (__bf16)f); }
__device__ __forceinline__ float bfr(float f) { return bf2f(f2bf(f)); }
__device__ __forceinline__ unsigned pack2(float lo, float hi) { return f2bf(lo) | (f2bf(hi) << 16); }
template <int CTRL> __device__ __forceinline__ float dpp(float x) {
  return __builtin_bit_cast(float, __builtin_amdgcn_mov_dpp(__builtin_bit_cast(int, x), CTRL, 0xf, 0xf, true));
}
constexpr int XOR1 = 0xB1, ROR1 = 0x121, ROR2 = 0x122, ROR4 = 0x124, ROR8 = 0x128;
__device__ __forceinline__ float row_max(float m) {
  m = fmaxf(m, dpp<ROR1>(m)); m = fmaxf(m, dpp<ROR2>(m)); m = fmaxf(m, dpp<ROR4>(m)); return fmaxf(m, dpp<ROR8>(m));
}
__device__ __forceinline__ float row_sum(float m) {
  m += dpp<ROR1>(m); m += dpp<ROR2>(m); m += dpp<ROR4>(m); return m + dpp<ROR8>(m);
}
__device__ __forceinline__ float wave_sum(float x) {
  x = row_sum(x);
  auto s = __builtin_amdgcn_permlane16_swap(__float_as_uint(x), __float_as_uint(x), false, false);
  x = __uint_as_float(s[0]) + __uint_as_float(s[1]);
  auto t = __builtin_amdgcn_permlane32_swap(__float_as_uint(x), __float_as_uint(x), false, false);
  return __uint_as_float(t[0]) + __uint_as_float(t[1]);
}
__device__ __forceinline__ void wave_lds_sync() {
  __builtin_amdgcn_fence(__ATOMIC_RELEASE, "wavefront");
  __builtin_amdgcn_wave_barrier();
  __builtin_amdgcn_fence(__ATOMIC_ACQUIRE, "wavefront");
}
__device__ __forceinline__ v4u ldnt(const void* p) { return __builtin_nontemporal_load(reinterpret_cast<const v4u*>(p)); }
__device__ __forceinline__ f32x4 mfma16(v4u a, v4u b, f32x4 c) {
  return __builtin_amdgcn_mfma_f32_16x16x32_bf16(__builtin_bit_cast(bf16x8, a), __builtin_bit_cast(bf16x8, b), c, 0, 0, 0);
}
__device__ __forceinline__ unsigned swz(unsigned row, unsigned ch) {
  return 256u * row + 16u * (ch ^ (((row & 3) << 2) | ((row >> 2) & 3)));
}

__device__ __forceinline__ void put_granule(__amdgpu_buffer_rsrc_t rs, unsigned off, unsigned w0, unsigned w1,
                                            unsigned tagA, unsigned tagB) {
  v4u g = {w0, tagA, w1, tagB};
  __builtin_amdgcn_raw_buffer_store_b128(g, rs, (int)off, 0, 16  );
}
template <int N>
__device__ __forceinline__ void load_granules(__amdgpu_buffer_rsrc_t rs, const unsigned (&off)[N], v4u (&g)[N]) {
#pragma unroll
  for (int n = 0; n < N; ++n) {
    unsigned o = off[n];
    asm volatile("" : "+v"(o)::"memory");
    g[n] = __builtin_amdgcn_raw_buffer_load_b128(rs, (int)o, 0, 16  );
  }
}
template <int N>
__device__ __forceinline__ bool granules_ok(const v4u (&g)[N], unsigned tagA, unsigned tagB) {
  bool ok = true;
#pragma unroll
  for (int n = 0; n < N; ++n) ok = ok && (g[n].y == tagA) && (g[n].w == tagB);
  return __all(ok);
}
template <int N>
__device__ __forceinline__ void get_granules(__amdgpu_buffer_rsrc_t rs, const unsigned (&off)[N], v4u (&g)[N],
                                             unsigned tagA, unsigned tagB) {
  for (unsigned spins = 0;; ++spins) {
    asm volatile("" ::: "memory");
    load_granules<N>(rs, off, g);
    if (granules_ok<N>(g, tagA, tagB) || spins > SPIN_CAP) break;
    __builtin_amdgcn_s_sleep(1);
  }
  asm volatile("" ::: "memory");
}

__global__ void __launch_bounds__(NTHR, 3)
fused_decode_kernel(const unsigned short* __restrict__ x, const unsigned short* __restrict__ kc_,
                    const unsigned short* __restrict__ vc_, const unsigned short* __restrict__ wq,
                    const unsigned short* __restrict__ wo, const unsigned short* __restrict__ w1,
                    const unsigned short* __restrict__ b1, const unsigned short* __restrict__ w2,
                    const unsigned short* __restrict__ b2, unsigned short* __restrict__ out,
                    unsigned char* ws) {
  __shared__ __attribute__((aligned(16))) unsigned char smem[L_END];
  const int tid = threadIdx.x, lane = tid & 63;
  const int wave = __builtin_amdgcn_readfirstlane(tid >> 6);
  const int wg = blockIdx.x;
  const int g4 = lane >> 4, i16 = lane & 15;
  const int tq = i16 >> 2, tp = lane & 3;
  const __amdgpu_buffer_rsrc_t rs = __builtin_amdgcn_make_buffer_rsrc((void*)ws, 0, (int)WS_END, 0x00020000);
  const int ab = wg >> 5, kvh = (wg >> 2) & 7, sp = wg & 3;
  STAMP_DECL;
  STAMP(0);

  if (EXP == 4 && wave >= 8) return;
  if (EXP == 7 && wave >= 8) { __builtin_amdgcn_s_barrier(); __builtin_amdgcn_s_barrier(); return; }
  if (wave >= 8) {
    __builtin_amdgcn_s_setprio(3);
    const int sv = wave - 8;
    const int cs = sv == 0 ? ((wg & 7) * 32 + (wg >> 3)) : ((wg & 7) * 14 + (wg >> 3));
    const bool has_w = (sv == 0) || ((wg >> 3) < 14);
    v4u xv[4], wa[16];
    unsigned epoch = __hip_atomic_load((gu32*)(ws + WS_E), __ATOMIC_RELAXED, __HIP_MEMORY_SCOPE_AGENT);
#pragma unroll
    for (int b = 0; b < 4; ++b) xv[b] = *reinterpret_cast<const v4u*>(x + (sv * 4 + b) * DM + lane * 8);
    {
      const __amdgpu_buffer_rsrc_t rsW = __builtin_amdgcn_make_buffer_rsrc(
          (void*)(sv == 0 ? wq : w1), 0, (has_w && EXP != 2) ? (sv == 0 ? DM * NQC * 2 : DM * FF * 2) : 0, 0x00020000);
      const int ldw = sv == 0 ? NQC : FF;
#pragma unroll
      for (int i = 0; i < 16; ++i) {
        const int pi = lane + 64 * i;
        wa[i] = __builtin_amdgcn_raw_buffer_load_b128(rsW, ((pi >> 1) * ldw + cs * 16 + (pi & 1) * 8) * 2, 0, WA_AUX);
      }
    }
    v2u b1v = *reinterpret_cast<const v2u*>(b1 + ((sv == 0 || !has_w) ? 0 : cs * 16 + g4 * 4));
    __builtin_amdgcn_sched_barrier(0);
    __builtin_amdgcn_s_barrier();
    __builtin_amdgcn_sched_barrier(0);
    const unsigned xn = L_XN, img = L_WA + sv * 16384;
#pragma unroll
    for (int b = 0; b < 4; ++b) {
      f32x2 e[4];
#pragma unroll
      for (int w = 0; w < 4; ++w) e[w] = f32x2{bflo(xv[b][w]), bfhi(xv[b][w])};
      const f32x2 s2 = (e[0] + e[1]) + (e[2] + e[3]);
      const float mean = wave_sum(s2.x + s2.y) * (1.0f / DM);
      f32x2 q2 = {0.f, 0.f};
#pragma unroll
      for (int w = 0; w < 4; ++w) { const f32x2 d = e[w] - mean; q2 += d * d; }
      const float var = wave_sum(q2.x + q2.y) * (1.0f / DM);
      const float mb = bfr(mean);
      const float rinv = __builtin_amdgcn_rcpf(bfr(__builtin_amdgcn_sqrtf(bfr(bfr(var) + 1e-5f))));
      v4u o;
#pragma unroll
      for (int w = 0; w < 4; ++w) {
        const f32x2 t = e[w] - mb;
        const unsigned tb = pack2(t.x, t.y);
        const f32x2 u = f32x2{bflo(tb), bfhi(tb)} * rinv;
        o[w] = pack2(u.x, u.y);
      }
      *reinterpret_cast<v4u*>(smem + xn + (sv * 4 + b) * XN_STR + lane * 16) = o;
    }
    STAMP(8);
    {
      __builtin_amdgcn_fence(__ATOMIC_RELEASE, "workgroup");
      LAS unsigned* cnt = (LAS unsigned*)(smem + L_CNT);
      if (lane == 0) __hip_atomic_fetch_add(cnt, 1u, __ATOMIC_RELAXED, __HIP_MEMORY_SCOPE_WORKGROUP);
      if (!has_w) { __builtin_amdgcn_s_barrier(); return; }
#pragma unroll
      for (int i = 0; i < 16; ++i) *reinterpret_cast<v4u*>(smem + img + (lane + 64 * i) * 16) = wa[i];
      while (__hip_atomic_load(cnt, __ATOMIC_RELAXED, __HIP_MEMORY_SCOPE_WORKGROUP) < 2u) __builtin_amdgcn_s_sleep(1);
      __builtin_amdgcn_fence(__ATOMIC_ACQUIRE, "workgroup");
    }
    asm volatile("" : "+v"(epoch), "+v"(b1v));
    epoch += 1u;
    const unsigned tagA = epoch, tagB = epoch ^ 0x9E3779B9u;
    __builtin_amdgcn_sched_barrier(0);
    STAMP(1);
    __builtin_amdgcn_sched_barrier(0);
    wave_lds_sync();
    v4u afr[16], bfr_[16];
#pragma unroll
    for (int ks = 0; ks < 16; ++ks) {
      const s16x4 a0 = __builtin_amdgcn_ds_read_tr16_b64_v4i16((lds_s16x4*)(smem + img + (32 * ks + 8 * g4 + tq) * 32 + tp * 8));
      const s16x4 a1 = __builtin_amdgcn_ds_read_tr16_b64_v4i16((lds_s16x4*)(smem + img + (32 * ks + 8 * g4 + 4 + tq) * 32 + tp * 8));
      const v2u u0 = __builtin_bit_cast(v2u, a0), u1 = __builtin_bit_cast(v2u, a1);
      afr[ks] = v4u{u0.x, u0.y, u1.x, u1.y};
      bfr_[ks] = *reinterpret_cast<const v4u*>(smem + xn + (i16 & 7) * XN_STR + (32 * ks + 8 * g4) * 2);
    }
    __builtin_amdgcn_sched_barrier(0);
    f32x4 ac4[4] = {{0.f, 0.f, 0.f, 0.f}, {0.f, 0.f, 0.f, 0.f}, {0.f, 0.f, 0.f, 0.f}, {0.f, 0.f, 0.f, 0.f}};
#pragma unroll
    for (int ks = 0; ks < 16; ++ks) ac4[ks & 3] = mfma16(afr[ks], bfr_[ks], ac4[ks & 3]);
    const f32x4 acc = (ac4[0] + ac4[1]) + (ac4[2] + ac4[3]);
    if (i16 < 8) {
      if (sv == 0) {
        put_granule(rs, WS_Q + (unsigned)(i16 * 1024 + cs * 4 + g4) * 16u, pack2(acc[0], acc[1]), pack2(acc[2], acc[3]), tagA, tagB);
      } else {
        float hv[4];
#pragma unroll
        for (int j = 0; j < 4; ++j) {
          const float bj = (j & 1) ? bfhi(b1v[j >> 1]) : bflo(b1v[j >> 1]);
          const float h = bfr(bfr(acc[j]) + bj);
          const float u = 0.7978845608028654f * (h + 0.044715f * h * h * h);
          hv[j] = 0.5f * h * (1.0f + tanhf(u));
        }
        put_granule(rs, WS_H + (unsigned)(i16 * 448 + cs * 4 + g4) * 16u, pack2(hv[0], hv[1]), pack2(hv[2], hv[3]), tagA, tagB);
      }
    }
    STAMP(2);
    if (sv == 0) {
      __builtin_amdgcn_s_sleep(QDELAY);
      unsigned qoff[2];
      v4u qg[2];
#pragma unroll
      for (int e = 0; e < 2; ++e) qoff[e] = WS_Q + (unsigned)(ab * 1024 + kvh * 128 + lane + 64 * e) * 16u;
      get_granules<2>(rs, qoff, qg, tagA, tagB);
#pragma unroll
      for (int e = 0; e < 2; ++e) *reinterpret_cast<v2u*>(smem + L_QS + (lane + 64 * e) * 8) = v2u{qg[e].x, qg[e].z};
      STAMP(9);
    }
    __syncthreads();
    return;
  }

  const int ckc = wg >> 2, cnc = wg & 3;
  const int d_n = (wg & 31) * 16 + i16;
  unsigned epoch = __hip_atomic_load((gu32*)(ws + WS_E), __ATOMIC_RELAXED, __HIP_MEMORY_SCOPE_AGENT);
  const unsigned d_b2 = b2[d_n];
  const unsigned d_x = x[(wg >> 5) * DM + d_n];
  if (tid == 0) __hip_atomic_store((LAS unsigned*)(smem + L_CNT), 0u, __ATOMIC_RELAXED, __HIP_MEMORY_SCOPE_WORKGROUP);
  __builtin_amdgcn_sched_barrier(0);
  asm volatile("s_waitcnt lgkmcnt(0)" ::: "memory");
  __builtin_amdgcn_s_barrier();
  __builtin_amdgcn_sched_barrier(0);
  v4u Kr[8], Vr[8], Wc[4];
  {
    const size_t kv0 = ((size_t)(ab * NHKV + kvh) * SK + (SK - WINDOW) + sp * 256 + wave * 32) * DH;
#pragma unroll
    for (int i = 0; i < 8; ++i) Kr[i] = ldnt(kc_ + kv0 + (size_t)(4 * i + g4) * DH + i16 * 8);
#pragma unroll
    for (int i = 0; i < 8; ++i) Vr[i] = ldnt(vc_ + kv0 + (size_t)(4 * i + g4) * DH + i16 * 8);
    const unsigned short* wb = (ckc < 32) ? (wo + (size_t)ckc * 128 * DM) : (w2 + (size_t)(ckc - 32) * 128 * DM);
    const __amdgpu_buffer_rsrc_t rsC =
        __builtin_amdgcn_make_buffer_rsrc((void*)wb, 0, (wg < N_CWG && EXP != 3) ? 128 * DM * 2 : 0, 0x00020000);
#pragma unroll
    for (int i = 0; i < 4; ++i)
      Wc[i] = __builtin_amdgcn_raw_buffer_load_b128(rsC, (((tid >> 4) + 32 * i) * DM + cnc * 128 + i16 * 8) * 2, 0, 2);
  }
  __builtin_amdgcn_sched_barrier(0);
  STAMP(1);
  __syncthreads();
  STAMP(2);
  asm volatile("" : "+v"(epoch));
  epoch += 1u;
  const unsigned tagA = epoch, tagB = epoch ^ 0x9E3779B9u;

  {
#pragma unroll
    for (int i = 0; i < 8; ++i)
      *reinterpret_cast<v4u*>(smem + L_VI + swz(wave * 32 + 4 * i + g4, i16)) = Kr[i];
    wave_lds_sync();
#pragma unroll
    for (int t = 0; t < 2; ++t)
#pragma unroll
      for (int kk = 0; kk < 4; ++kk)
        Kr[t * 4 + kk] = *reinterpret_cast<const v4u*>(smem + L_VI + swz(wave * 32 + 16 * t + i16, 4 * kk + g4));
    f32x4 sc[2];
#pragma unroll
    for (int t = 0; t < 2; ++t) {
      f32x4 a = {0.f, 0.f, 0.f, 0.f};
#pragma unroll
      for (int kk = 0; kk < 4; ++kk) {
        const v4u qa = *reinterpret_cast<const v4u*>(smem + L_QS + ((i16 & 3) * 128 + 32 * kk + 8 * g4) * 2);
        a = mfma16(qa, Kr[t * 4 + kk], a);
      }
#pragma unroll
      for (int h = 0; h < 4; ++h) sc[t][h] = bfr(bfr(a[h]) * 0.08838834764831845f) * 1.4426950408889634f;
    }
    if (g4 == 0) {
      f32x4 mw;
#pragma unroll
      for (int h = 0; h < 4; ++h) mw[h] = row_max(fmaxf(sc[0][h], sc[1][h]));
      if (i16 == 0) *reinterpret_cast<f32x4*>(smem + L_MX + wave * 16) = mw;
    }
    STAMP(3);
    __syncthreads();
    __builtin_amdgcn_sched_barrier(0);
    STAMP(8);
    __builtin_amdgcn_sched_barrier(0);
    f32x4 M = *reinterpret_cast<const f32x4*>(smem + L_MX);
#pragma unroll
    for (int w = 1; w < 8; ++w) {
      const f32x4 m2 = *reinterpret_cast<const f32x4*>(smem + L_MX + w * 16);
#pragma unroll
      for (int h = 0; h < 4; ++h) M[h] = fmaxf(M[h], m2[h]);
    }
    if (g4 == 0) {
      f32x4 lw;
#pragma unroll
      for (int h = 0; h < 4; ++h) {
        float l = 0.f;
#pragma unroll
        for (int t = 0; t < 2; ++t) {
          const unsigned pb = f2bf(__builtin_amdgcn_exp2f(sc[t][h] - M[h]));
          *reinterpret_cast<unsigned short*>(smem + L_P + h * P_STR + (wave * 32 + t * 16 + i16) * 2) = (unsigned short)pb;
          l += bf2f(pb);
        }
        lw[h] = row_sum(l);
      }
      if (i16 == 0) *reinterpret_cast<f32x4*>(smem + L_LS + wave * 16) = lw;
    }
    __builtin_amdgcn_sched_barrier(0);
    STAMP(9);
    __builtin_amdgcn_sched_barrier(0);
#pragma unroll
    for (int i = 0; i < 8; ++i)
      *reinterpret_cast<v4u*>(smem + L_VI + swz(wave * 32 + 4 * i + g4, i16)) = Vr[i];
    STAMP(10);
    __syncthreads();
    v4u pa[8], vb[8];
#pragma unroll
    for (int ks = 0; ks < 8; ++ks) {
      pa[ks] = *reinterpret_cast<const v4u*>(smem + L_P + (i16 & 3) * P_STR + (32 * ks + 8 * g4) * 2);
      const s16x4 b0 = __builtin_amdgcn_ds_read_tr16_b64_v4i16(
          (lds_s16x4*)(smem + L_VI + swz(32 * ks + 8 * g4 + tq, 2 * wave + (tp >> 1)) + 8 * (tp & 1)));
      const s16x4 b1_ = __builtin_amdgcn_ds_read_tr16_b64_v4i16(
          (lds_s16x4*)(smem + L_VI + swz(32 * ks + 8 * g4 + 4 + tq, 2 * wave + (tp >> 1)) + 8 * (tp & 1)));
      const v2u u0 = __builtin_bit_cast(v2u, b0), u1 = __builtin_bit_cast(v2u, b1_);
      vb[ks] = v4u{u0.x, u0.y, u1.x, u1.y};
    }
    __builtin_amdgcn_sched_barrier(0);
    f32x4 o2[2] = {{0.f, 0.f, 0.f, 0.f}, {0.f, 0.f, 0.f, 0.f}};
#pragma unroll
    for (int ks = 0; ks < 8; ++ks) o2[ks & 1] = mfma16(pa[ks], vb[ks], o2[ks & 1]);
    const f32x4 o = o2[0] + o2[1];
    const float og = g4 == 0 ? o[0] : g4 == 1 ? o[1] : g4 == 2 ? o[2] : o[3];
    const float on = dpp<XOR1>(og);
    const unsigned slot = (unsigned)(((kvh * 4 + g4) * 8 + ab) * 4 + sp);
    if ((i16 & 1) == 0)
      put_granule(rs, WS_O + (slot * 64u + (unsigned)(wave * 8 + (i16 >> 1))) * 16u, __float_as_uint(og), __float_as_uint(on), tagA, tagB);
    if (wave == 0 && i16 == 0) {
      float Ls = 0.f;
#pragma unroll
      for (int w = 0; w < 8; ++w) Ls += *reinterpret_cast<const float*>(smem + L_LS + w * 16 + g4 * 4);
      const float Mg = g4 == 0 ? M[0] : g4 == 1 ? M[1] : g4 == 2 ? M[2] : M[3];
      put_granule(rs, WS_ML + slot * 16u, __float_as_uint(Mg), __float_as_uint(Ls), tagA, tagB);
    }
  }
  STAMP(4);

  if (wg < N_CWG) {
#pragma unroll
    for (int i = 0; i < 4; ++i) *reinterpret_cast<v4u*>(smem + L_WC + swz((tid >> 4) + 32 * i, i16)) = Wc[i];
    __syncthreads();
    v4u af[4];
#pragma unroll
    for (int ks = 0; ks < 4; ++ks) {
      const s16x4 a0 = __builtin_amdgcn_ds_read_tr16_b64_v4i16(
          (lds_s16x4*)(smem + L_WC + swz(32 * ks + 8 * g4 + tq, 2 * wave + (tp >> 1)) + 8 * (tp & 1)));
      const s16x4 a1 = __builtin_amdgcn_ds_read_tr16_b64_v4i16(
          (lds_s16x4*)(smem + L_WC + swz(32 * ks + 8 * g4 + 4 + tq, 2 * wave + (tp >> 1)) + 8 * (tp & 1)));
      const v2u u0 = __builtin_bit_cast(v2u, a0), u1 = __builtin_bit_cast(v2u, a1);
      af[ks] = v4u{u0.x, u0.y, u1.x, u1.y};
    }
    if (ckc < 32) {
      unsigned off[8];
      v4u g[8];
#pragma unroll
      for (int s = 0; s < 4; ++s) {
        const unsigned slot = (unsigned)((ckc * 8 + wave) * 4 + s);
        off[s] = WS_O + (slot * 64u + lane) * 16u;
        off[4 + s] = WS_ML + slot * 16u;
      }
      get_granules<8>(rs, off, g, tagA, tagB);
      float M = __uint_as_float(g[4].x);
#pragma unroll
      for (int s = 1; s < 4; ++s) M = fmaxf(M, __uint_as_float(g[4 + s].x));
      float Ls = 0.f, o0 = 0.f, o1 = 0.f;
#pragma unroll
      for (int s = 0; s < 4; ++s) {
        const float wt = __builtin_amdgcn_exp2f(__uint_as_float(g[4 + s].x) - M);
        Ls = fmaf(wt, __uint_as_float(g[4 + s].z), Ls);
        o0 = fmaf(wt, __uint_as_float(g[s].x), o0);
        o1 = fmaf(wt, __uint_as_float(g[s].z), o1);
      }
      *reinterpret_cast<unsigned*>(smem + L_ACT + wave * ACT_STR + lane * 4) = pack2(o0 / Ls, o1 / Ls);
      STAMP(5);
    } else if (lane < 32) {
      unsigned off[1] = {WS_H + (unsigned)(wave * 448 + (ckc - 32) * 32 + lane) * 16u};
      v4u g[1];
      get_granules<1>(rs, off, g, tagA, tagB);
      *reinterpret_cast<v2u*>(smem + L_ACT + wave * ACT_STR + lane * 8) = v2u{g[0].x, g[0].z};
    }
    __syncthreads();
    f32x4 acc = {0.f, 0.f, 0.f, 0.f};
#pragma unroll
    for (int ks = 0; ks < 4; ++ks) {
      const v4u bfg = *reinterpret_cast<const v4u*>(smem + L_ACT + (i16 & 7) * ACT_STR + (32 * ks + 8 * g4) * 2);
      acc = mfma16(af[ks], bfg, acc);
    }
    if (i16 < 8) {
      const unsigned po = WS_P + (unsigned)((ckc * 8 + i16) * 256 + cnc * 64 + wave * 8 + g4 * 2) * 16u;
      put_granule(rs, po, __float_as_uint(acc[0]), __float_as_uint(acc[1]), tagA, tagB);
      put_granule(rs, po + 16u, __float_as_uint(acc[2]), __float_as_uint(acc[3]), tagA, tagB);
    }
  }
  STAMP(6);

  {
    const int db = wg >> 5, dg0 = (wg & 31) * 8;
    if (tid < N_KC * 8) {
      const int kc = tid >> 3, gq = tid & 7;
      unsigned off[1] = {WS_P + (unsigned)((kc * 8 + db) * 256 + dg0 + gq) * 16u};
      v4u g[1];
      get_granules<1>(rs, off, g, tagA, tagB);
      *reinterpret_cast<v2u*>(smem + L_PART + kc * 64 + gq * 8) = v2u{g[0].x, g[0].z};
    }
    __syncthreads();
    STAMP(7);
    if (tid < 16) {
      const int n = dg0 * 2 + tid;
      float sa = 0.f, sm = 0.f;
#pragma unroll
      for (int k = 0; k < 32; ++k) sa += *reinterpret_cast<const float*>(smem + L_PART + k * 64 + tid * 4);
#pragma unroll
      for (int k = 32; k < N_KC; ++k) sm += *reinterpret_cast<const float*>(smem + L_PART + k * 64 + tid * 4);
      const float mlp = bfr(bfr(sm) + bf2f(d_b2));
      const float y = bfr(bfr(bf2f(d_x) + bfr(sa)) + mlp);
      out[db * DM + n] = (unsigned short)f2bf(y);
    }
    if (wg == 0 && tid == 0) __hip_atomic_store((gu32*)(ws + WS_E), epoch, __ATOMIC_RELAXED, __HIP_MEMORY_SCOPE_AGENT);
  }
  STAMP_FLUSH;
}

}

extern "C" void kernel_launch(void* const* d_in, const int* in_sizes, int n_in, void* d_out, int out_size,
                              void* d_ws, size_t ws_size, hipStream_t stream) {
  (void)in_sizes; (void)n_in; (void)out_size; (void)ws_size;
  const unsigned short* x = (const unsigned short*)d_in[0];
  const unsigned short* k = (const unsigned short*)d_in[1];
  const unsigned short* v = (const unsigned short*)d_in[2];
  const unsigned short* wq = (const unsigned short*)d_in[3];
  const unsigned short* wo = (const unsigned short*)d_in[4];
  const unsigned short* w1 = (const unsigned short*)d_in[6];
  const unsigned short* b1 = (const unsigned short*)d_in[7];
  const unsigned short* w2 = (const unsigned short*)d_in[8];
  const unsigned short* b2 = (const unsigned short*)d_in[9];
  hipLaunchKernelGGL(fused_decode_kernel, dim3(NWG), dim3(NTHR), 0, stream, x, k, v, wq, wo, w1, b1, w2, b2,
                     (unsigned short*)d_out, (unsigned char*)d_ws);
}
```

```cpp
#include <hip/hip_runtime.h>
#include <hip/hip_bf16.h>
#include <stdint.h>

namespace {

constexpr int DM = 512, NHKV = 8, SK = 4096, DH = 128, WINDOW = 1024, FF = 1792;
constexpr int NWG = 256, NTHR = 640;
constexpr int NQC = 4096;
constexpr int N_HT = (FF / 64) * 4;
constexpr int N_KC = 32 + FF / 128;
constexpr int N_CWG = N_KC * 4;
#define QDELAY 8

typedef unsigned v4u __attribute__((ext_vector_type(4)));
typedef unsigned v2u __attribute__((ext_vector_type(2)));
typedef float f32x4 __attribute__((ext_vector_type(4)));
typedef float f32x2 __attribute__((ext_vector_type(2)));
typedef short bf16x8 __attribute__((ext_vector_type(8)));
typedef short s16x4 __attribute__((ext_vector_type(4)));
typedef __attribute__((address_space(1))) unsigned gu32;
typedef __attribute__((address_space(3))) s16x4 lds_s16x4;
#define LAS __attribute__((address_space(3)))

constexpr unsigned WS_E = 0;
constexpr unsigned WS_Q = 4096;
constexpr unsigned WS_H = WS_Q + 4 * 8 * 2048 * 16;
constexpr unsigned WS_O = WS_H + 4 * 8 * 896 * 16;
constexpr unsigned WS_ML = WS_O + 32 * 8 * 4 * 64 * 16;
constexpr unsigned WS_P = WS_ML + 32 * 8 * 4 * 16;
constexpr unsigned WS_END = WS_P + N_KC * 8 * 256 * 16;

#define STAMP(i) do {} while (0)
#define STAMP_DECL do {} while (0)
#define STAMP_FLUSH do {} while (0)
constexpr unsigned SPIN_CAP = 1u << 18;

constexpr unsigned XN_STR = 1040, WA_STR = 144, P_STR = 528, ACT_STR = 272;
constexpr unsigned L_XN = 0;
constexpr unsigned L_WA = L_XN + 8 * XN_STR;
constexpr unsigned L_VI = L_WA + 2 * 128 * WA_STR;
constexpr unsigned L_WC = L_VI + 65536;
constexpr unsigned L_P = L_WC + 32768;
constexpr unsigned L_MX = L_P + 4 * P_STR;
constexpr unsigned L_LS = L_MX + 128;
constexpr unsigned L_ACT = L_LS + 128;
constexpr unsigned L_PART = L_ACT + 8 * ACT_STR;
constexpr unsigned L_QS = L_PART + N_KC * 64;
constexpr unsigned L_CNT = L_QS + 1024;
constexpr unsigned L_DBG = L_CNT + 16;
constexpr unsigned L_END = L_DBG + 128;
static_assert(L_VI % 16 == 0 && L_WC % 16 == 0 && L_P % 16 == 0 && L_ACT % 16 == 0 && L_QS % 16 == 0, "align");

__device__ __forceinline__ float bf2f(unsigned bits16) { return __builtin_bit_cast(float, bits16 << 16); }
__device__ __forceinline__ float bflo(unsigned w) { return __builtin_bit_cast(float, w << 16); }
__device__ __forceinline__ float bfhi(unsigned w) { return __builtin_bit_cast(float, w & 0xffff0000u); }
__device__ __forceinline__ unsigned f2bf(float f) { return (unsigned)__builtin_bit_cast(unsigned short, (__bf16)f); }
__device__ __forceinline__ float bfr(float f) { return bf2f(f2bf(f)); }
__device__ __forceinline__ unsigned pack2(float lo, float hi) { return f2bf(lo) | (f2bf(hi) << 16); }
template <int CTRL> __device__ __forceinline__ float dpp(float x) {
  return __builtin_bit_cast(float, __builtin_amdgcn_mov_dpp(__builtin_bit_cast(int, x), CTRL, 0xf, 0xf, true));
}
constexpr int XOR1 = 0xB1, ROR1 = 0x121, ROR2 = 0x122, ROR4 = 0x124, ROR8 = 0x128;
__device__ __forceinline__ float row_max(float m) {
  m = fmaxf(m, dpp<ROR1>(m)); m = fmaxf(m, dpp<ROR2>(m)); m = fmaxf(m, dpp<ROR4>(m)); return fmaxf(m, dpp<ROR8>(m));
}
__device__ __forceinline__ float row_sum(float m) {
  m += dpp<ROR1>(m); m += dpp<ROR2>(m); m += dpp<ROR4>(m); return m + dpp<ROR8>(m);
}
__device__ __forceinline__ float wave_sum(float x) {
  x = row_sum(x);
  auto s = __builtin_amdgcn_permlane16_swap(__float_as_uint(x), __float_as_uint(x), false, false);
  x = __uint_as_float(s[0]) + __uint_as_float(s[1]);
  auto t = __builtin_amdgcn_permlane32_swap(__float_as_uint(x), __float_as_uint(x), false, false);
  return __uint_as_float(t[0]) + __uint_as_float(t[1]);
}
__device__ __forceinline__ void wave_lds_sync() {
  __builtin_amdgcn_fence(__ATOMIC_RELEASE, "wavefront");
  __builtin_amdgcn_wave_barrier();
  __builtin_amdgcn_fence(__ATOMIC_ACQUIRE, "wavefront");
}
__device__ __forceinline__ v4u ldnt(const void* p) { return __builtin_nontemporal_load(reinterpret_cast<const v4u*>(p)); }
__device__ __forceinline__ f32x4 mfma16(v4u a, v4u b, f32x4 c) {
  return __builtin_amdgcn_mfma_f32_16x16x32_bf16(__builtin_bit_cast(bf16x8, a), __builtin_bit_cast(bf16x8, b), c, 0, 0, 0);
}
__device__ __forceinline__ v4u tr_frag(unsigned char* smem, unsigned off0, unsigned off1) {
  const v2u u0 = __builtin_bit_cast(v2u, __builtin_amdgcn_ds_read_tr16_b64_v4i16((lds_s16x4*)(smem + off0)));
  const v2u u1 = __builtin_bit_cast(v2u, __builtin_amdgcn_ds_read_tr16_b64_v4i16((lds_s16x4*)(smem + off1)));
  return v4u{u0.x, u0.y, u1.x, u1.y};
}
__device__ __forceinline__ unsigned swz(unsigned row, unsigned ch) {
  return 256u * row + 16u * (ch ^ (((row & 3) << 2) | ((row >> 2) & 3)));
}
__device__ __forceinline__ float gelu_tanh(float h) {
  const float u = 0.7978845608028654f * (h + 0.044715f * h * h * h);
  return 0.5f * h * (1.0f + tanhf(u));
}

__device__ __forceinline__ void put_granule(__amdgpu_buffer_rsrc_t rs, unsigned off, float v0, float v1,
                                            unsigned tagA, unsigned tagB) {
  v4u g = {__float_as_uint(v0), tagA, __float_as_uint(v1), tagB};
  __builtin_amdgcn_raw_buffer_store_b128(g, rs, (int)off, 0, 16  );
}
template <int N>
__device__ __forceinline__ void get_granules(__amdgpu_buffer_rsrc_t rs, const unsigned (&off)[N], v4u (&g)[N],
                                             unsigned tagA, unsigned tagB) {
  for (unsigned spins = 0;; ++spins) {
#pragma unroll
    for (int n = 0; n < N; ++n) {
      unsigned o = off[n];
      asm volatile("" : "+v"(o)::"memory");
      g[n] = __builtin_amdgcn_raw_buffer_load_b128(rs, (int)o, 0, 16  );
    }
    bool ok = true;
#pragma unroll
    for (int n = 0; n < N; ++n) ok = ok && (g[n].y == tagA) && (g[n].w == tagB);
    if (__all(ok) || spins > SPIN_CAP) break;
    __builtin_amdgcn_s_sleep(1);
  }
  asm volatile("" ::: "memory");
}

__global__ void __launch_bounds__(NTHR, 3)
fused_decode_kernel(const unsigned short* __restrict__ x, const unsigned short* __restrict__ kc_,
                    const unsigned short* __restrict__ vc_, const unsigned short* __restrict__ wq,
                    const unsigned short* __restrict__ wo, const unsigned short* __restrict__ w1,
                    const unsigned short* __restrict__ b1, const unsigned short* __restrict__ w2,
                    const unsigned short* __restrict__ b2, unsigned short* __restrict__ out,
                    unsigned char* ws) {
  __shared__ __attribute__((aligned(16))) unsigned char smem[L_END];
  const int tid = threadIdx.x, lane = tid & 63;
  const int wave = __builtin_amdgcn_readfirstlane(tid >> 6);
  const int wg = blockIdx.x;
  const int g4 = lane >> 4, i16 = lane & 15;
  const int tq = i16 >> 2, tp = lane & 3;
  const __amdgpu_buffer_rsrc_t rs = __builtin_amdgcn_make_buffer_rsrc((void*)ws, 0, (int)WS_END, 0x00020000);
  const int ab = wg >> 5, kvh = (wg >> 2) & 7, sp = wg & 3;
  STAMP_DECL;
  STAMP(0);

  if (wave >= 8) {
    __builtin_amdgcn_s_setprio(3);
    const int sv = wave - 8;
    const int ht = wg - (NWG - N_HT);
    const bool has_w = (sv == 0) || (ht >= 0);
    const int tile = sv == 0 ? wg : ht;
    const int cb = tile >> 2, rb = tile & 3;
    v4u xv[4], wa[16];
    unsigned epoch = __hip_atomic_load((gu32*)(ws + WS_E), __ATOMIC_RELAXED, __HIP_MEMORY_SCOPE_AGENT);
#pragma unroll
    for (int b = 0; b < 4; ++b) xv[b] = *reinterpret_cast<const v4u*>(x + (sv * 4 + b) * DM + lane * 8);
    {
      const __amdgpu_buffer_rsrc_t rsW = __builtin_amdgcn_make_buffer_rsrc(
          (void*)(sv == 0 ? wq : w1), 0, has_w ? (sv == 0 ? DM * NQC * 2 : DM * FF * 2) : 0, 0x00020000);
      const int ldw = sv == 0 ? NQC : FF;
#pragma unroll
      for (int i = 0; i < 16; ++i) {
        const int pi = lane + 64 * i;
        wa[i] = __builtin_amdgcn_raw_buffer_load_b128(rsW, ((rb * 128 + (pi >> 3)) * ldw + cb * 64 + (pi & 7) * 8) * 2, 0, 2);
      }
    }
    __builtin_amdgcn_sched_barrier(0);
    __builtin_amdgcn_s_barrier();
    __builtin_amdgcn_sched_barrier(0);
    const unsigned img = L_WA + sv * (128 * WA_STR);
#pragma unroll
    for (int b = 0; b < 4; ++b) {
      f32x2 e[4];
#pragma unroll
      for (int w = 0; w < 4; ++w) e[w] = f32x2{bflo(xv[b][w]), bfhi(xv[b][w])};
      const f32x2 s2 = (e[0] + e[1]) + (e[2] + e[3]);
      const float mean = wave_sum(s2.x + s2.y) * (1.0f / DM);
      f32x2 q2 = {0.f, 0.f};
#pragma unroll
      for (int w = 0; w < 4; ++w) { const f32x2 d = e[w] - mean; q2 += d * d; }
      const float var = wave_sum(q2.x + q2.y) * (1.0f / DM);
      const float mb = bfr(mean);
      const float rinv = __builtin_amdgcn_rcpf(bfr(__builtin_amdgcn_sqrtf(bfr(bfr(var) + 1e-5f))));
      v4u o;
#pragma unroll
      for (int w = 0; w < 4; ++w) {
        const f32x2 t = e[w] - mb;
        const unsigned tb = pack2(t.x, t.y);
        const f32x2 u = f32x2{bflo(tb), bfhi(tb)} * rinv;
        o[w] = pack2(u.x, u.y);
      }
      *reinterpret_cast<v4u*>(smem + L_XN + (sv * 4 + b) * XN_STR + lane * 16) = o;
    }
    STAMP(8);
    {
      __builtin_amdgcn_fence(__ATOMIC_RELEASE, "workgroup");
      LAS unsigned* cnt = (LAS unsigned*)(smem + L_CNT);
      if (lane == 0) __hip_atomic_fetch_add(cnt, 1u, __ATOMIC_RELAXED, __HIP_MEMORY_SCOPE_WORKGROUP);
      if (!has_w) { __builtin_amdgcn_s_barrier(); return; }
#pragma unroll
      for (int i = 0; i < 16; ++i) {
        const int pi = lane + 64 * i;
        *reinterpret_cast<v4u*>(smem + img + (pi >> 3) * WA_STR + (pi & 7) * 16) = wa[i];
      }
      while (__hip_atomic_load(cnt, __ATOMIC_RELAXED, __HIP_MEMORY_SCOPE_WORKGROUP) < 2u) __builtin_amdgcn_s_sleep(1);
      __builtin_amdgcn_fence(__ATOMIC_ACQUIRE, "workgroup");
    }
    asm volatile("" : "+v"(epoch));
    epoch += 1u;
    const unsigned tagA = epoch, tagB = epoch ^ 0x9E3779B9u;
    STAMP(1);
    wave_lds_sync();
    {
      v4u afr[16], bfg[4];
#pragma unroll
      for (int ks = 0; ks < 4; ++ks) {
        bfg[ks] = *reinterpret_cast<const v4u*>(smem + L_XN + (i16 & 7) * XN_STR + (rb * 128 + 32 * ks + 8 * g4) * 2);
#pragma unroll
        for (int nt = 0; nt < 4; ++nt)
          afr[nt * 4 + ks] = tr_frag(smem, img + (32 * ks + 8 * g4 + tq) * WA_STR + nt * 32 + tp * 8,
                                     img + (32 * ks + 8 * g4 + 4 + tq) * WA_STR + nt * 32 + tp * 8);
      }
      __builtin_amdgcn_sched_barrier(0);
      f32x4 acc[4] = {{0.f, 0.f, 0.f, 0.f}, {0.f, 0.f, 0.f, 0.f}, {0.f, 0.f, 0.f, 0.f}, {0.f, 0.f, 0.f, 0.f}};
#pragma unroll
      for (int ks = 0; ks < 4; ++ks)
#pragma unroll
        for (int nt = 0; nt < 4; ++nt) acc[nt] = mfma16(afr[nt * 4 + ks], bfg[ks], acc[nt]);
      if (i16 < 8) {
        const unsigned base = sv == 0 ? WS_Q + (unsigned)((rb * 8 + i16) * 2048 + cb * 32 + g4 * 2) * 16u
                                      : WS_H + (unsigned)((rb * 8 + i16) * 896 + cb * 32 + g4 * 2) * 16u;
#pragma unroll
        for (int nt = 0; nt < 4; ++nt) {
          put_granule(rs, base + nt * 128u, acc[nt][0], acc[nt][1], tagA, tagB);
          put_granule(rs, base + nt * 128u + 16u, acc[nt][2], acc[nt][3], tagA, tagB);
        }
      }
    }
    STAMP(2);
    if (sv == 0) {
      __builtin_amdgcn_s_sleep(QDELAY);
      unsigned qoff[16];
      v4u qg[16];
#pragma unroll
      for (int e = 0; e < 4; ++e)
#pragma unroll
        for (int r = 0; r < 4; ++r)
          qoff[e * 4 + r] = WS_Q + (unsigned)((r * 8 + ab) * 2048 + kvh * 256 + lane + 64 * e) * 16u;
      get_granules<16>(rs, qoff, qg, tagA, tagB);
#pragma unroll
      for (int e = 0; e < 4; ++e) {
        const float s0 = (__uint_as_float(qg[e * 4].x) + __uint_as_float(qg[e * 4 + 1].x)) +
                         (__uint_as_float(qg[e * 4 + 2].x) + __uint_as_float(qg[e * 4 + 3].x));
        const float s1 = (__uint_as_float(qg[e * 4].z) + __uint_as_float(qg[e * 4 + 1].z)) +
                         (__uint_as_float(qg[e * 4 + 2].z) + __uint_as_float(qg[e * 4 + 3].z));
        *reinterpret_cast<unsigned*>(smem + L_QS + (lane + 64 * e) * 4) = pack2(s0, s1);
      }
      STAMP(9);
    }
    __syncthreads();
    return;
  }

  const int ckc = wg >> 2, cnc = wg & 3;
  const int d_n = (wg & 31) * 16 + i16;
  unsigned epoch = __hip_atomic_load((gu32*)(ws + WS_E), __ATOMIC_RELAXED, __HIP_MEMORY_SCOPE_AGENT);
  const unsigned d_b2 = b2[d_n];
  const unsigned d_x = x[(wg >> 5) * DM + d_n];
  const unsigned d_b1 = *reinterpret_cast<const unsigned*>(b1 + ((ckc >= 32 && ckc < N_KC) ? (ckc - 32) * 128 + 2 * lane : 0));
  if (tid == 0) __hip_atomic_store((LAS unsigned*)(smem + L_CNT), 0u, __ATOMIC_RELAXED, __HIP_MEMORY_SCOPE_WORKGROUP);
  __builtin_amdgcn_sched_barrier(0);
  asm volatile("s_waitcnt lgkmcnt(0)" ::: "memory");
  __builtin_amdgcn_s_barrier();
  __builtin_amdgcn_sched_barrier(0);
  v4u Kr[8], Vr[8], Wc[4];
  {
    const size_t kv0 = ((size_t)(ab * NHKV + kvh) * SK + (SK - WINDOW) + sp * 256 + wave * 32) * DH;
#pragma unroll
    for (int t = 0; t < 2; ++t)
#pragma unroll
      for (int kk = 0; kk < 4; ++kk) Kr[t * 4 + kk] = ldnt(kc_ + kv0 + (size_t)(16 * t + i16) * DH + 32 * kk + 8 * g4);
#pragma unroll
    for (int i = 0; i < 8; ++i) Vr[i] = ldnt(vc_ + kv0 + (size_t)(4 * i + g4) * DH + i16 * 8);
    const unsigned short* wb = (ckc < 32) ? (wo + (size_t)ckc * 128 * DM) : (w2 + (size_t)(ckc - 32) * 128 * DM);
    const __amdgpu_buffer_rsrc_t rsC =
        __builtin_amdgcn_make_buffer_rsrc((void*)wb, 0, wg < N_CWG ? 128 * DM * 2 : 0, 0x00020000);
#pragma unroll
    for (int i = 0; i < 4; ++i)
      Wc[i] = __builtin_amdgcn_raw_buffer_load_b128(rsC, (((tid >> 4) + 32 * i) * DM + cnc * 128 + i16 * 8) * 2, 0, 2);
  }
  STAMP(1);
  __syncthreads();
  STAMP(2);
  asm volatile("" : "+v"(epoch));
  epoch += 1u;
  const unsigned tagA = epoch, tagB = epoch ^ 0x9E3779B9u;

  {
    f32x4 sc[2];
#pragma unroll
    for (int t = 0; t < 2; ++t) {
      f32x4 a = {0.f, 0.f, 0.f, 0.f};
#pragma unroll
      for (int kk = 0; kk < 4; ++kk) {
        const v4u qa = *reinterpret_cast<const v4u*>(smem + L_QS + ((i16 & 3) * 128 + 32 * kk + 8 * g4) * 2);
        a = mfma16(qa, Kr[t * 4 + kk], a);
      }
#pragma unroll
      for (int h = 0; h < 4; ++h) sc[t][h] = bfr(bfr(a[h]) * 0.08838834764831845f) * 1.4426950408889634f;
    }
    if (g4 == 0) {
      f32x4 mw;
#pragma unroll
      for (int h = 0; h < 4; ++h) mw[h] = row_max(fmaxf(sc[0][h], sc[1][h]));
      if (i16 == 0) *reinterpret_cast<f32x4*>(smem + L_MX + wave * 16) = mw;
    }
    STAMP(3);
    __syncthreads();
    f32x4 M = *reinterpret_cast<const f32x4*>(smem + L_MX);
#pragma unroll
    for (int w = 1; w < 8; ++w) {
      const f32x4 m2 = *reinterpret_cast<const f32x4*>(smem + L_MX + w * 16);
#pragma unroll
      for (int h = 0; h < 4; ++h) M[h] = fmaxf(M[h], m2[h]);
    }
    if (g4 == 0) {
      f32x4 lw;
#pragma unroll
      for (int h = 0; h < 4; ++h) {
        float l = 0.f;
#pragma unroll
        for (int t = 0; t < 2; ++t) {
          const unsigned pb = f2bf(__builtin_amdgcn_exp2f(sc[t][h] - M[h]));
          *reinterpret_cast<unsigned short*>(smem + L_P + h * P_STR + (wave * 32 + t * 16 + i16) * 2) = (unsigned short)pb;
          l += bf2f(pb);
        }
        lw[h] = row_sum(l);
      }
      if (i16 == 0) *reinterpret_cast<f32x4*>(smem + L_LS + wave * 16) = lw;
    }
#pragma unroll
    for (int i = 0; i < 8; ++i)
      *reinterpret_cast<v4u*>(smem + L_VI + swz(wave * 32 + 4 * i + g4, i16)) = Vr[i];
    STAMP(10);
    __syncthreads();
    v4u pa[8], vb[8];
#pragma unroll
    for (int ks = 0; ks < 8; ++ks) {
      pa[ks] = *reinterpret_cast<const v4u*>(smem + L_P + (i16 & 3) * P_STR + (32 * ks + 8 * g4) * 2);
      vb[ks] = tr_frag(smem, L_VI + swz(32 * ks + 8 * g4 + tq, 2 * wave + (tp >> 1)) + 8 * (tp & 1),
                       L_VI + swz(32 * ks + 8 * g4 + 4 + tq, 2 * wave + (tp >> 1)) + 8 * (tp & 1));
    }
    __builtin_amdgcn_sched_barrier(0);
    f32x4 o2[2] = {{0.f, 0.f, 0.f, 0.f}, {0.f, 0.f, 0.f, 0.f}};
#pragma unroll
    for (int ks = 0; ks < 8; ++ks) o2[ks & 1] = mfma16(pa[ks], vb[ks], o2[ks & 1]);
    const f32x4 o = o2[0] + o2[1];
    const float og = g4 == 0 ? o[0] : g4 == 1 ? o[1] : g4 == 2 ? o[2] : o[3];
    const float on = dpp<XOR1>(og);
    const unsigned slot = (unsigned)(((kvh * 4 + g4) * 8 + ab) * 4 + sp);
    if ((i16 & 1) == 0) put_granule(rs, WS_O + (slot * 64u + (unsigned)(wave * 8 + (i16 >> 1))) * 16u, og, on, tagA, tagB);
    if (wave == 0 && i16 == 0) {
      float Ls = 0.f;
#pragma unroll
      for (int w = 0; w < 8; ++w) Ls += *reinterpret_cast<const float*>(smem + L_LS + w * 16 + g4 * 4);
      const float Mg = g4 == 0 ? M[0] : g4 == 1 ? M[1] : g4 == 2 ? M[2] : M[3];
      put_granule(rs, WS_ML + slot * 16u, Mg, Ls, tagA, tagB);
    }
  }
  STAMP(4);

  if (wg < N_CWG) {
#pragma unroll
    for (int i = 0; i < 4; ++i) *reinterpret_cast<v4u*>(smem + L_WC + swz((tid >> 4) + 32 * i, i16)) = Wc[i];
    __syncthreads();
    v4u af[4];
#pragma unroll
    for (int ks = 0; ks < 4; ++ks)
      af[ks] = tr_frag(smem, L_WC + swz(32 * ks + 8 * g4 + tq, 2 * wave + (tp >> 1)) + 8 * (tp & 1),
                       L_WC + swz(32 * ks + 8 * g4 + 4 + tq, 2 * wave + (tp >> 1)) + 8 * (tp & 1));
    if (ckc < 32) {
      unsigned off[8];
      v4u g[8];
#pragma unroll
      for (int s = 0; s < 4; ++s) {
        const unsigned slot = (unsigned)((ckc * 8 + wave) * 4 + s);
        off[s] = WS_O + (slot * 64u + lane) * 16u;
        off[4 + s] = WS_ML + slot * 16u;
      }
      get_granules<8>(rs, off, g, tagA, tagB);
      float M = __uint_as_float(g[4].x);
#pragma unroll
      for (int s = 1; s < 4; ++s) M = fmaxf(M, __uint_as_float(g[4 + s].x));
      float Ls = 0.f, o0 = 0.f, o1 = 0.f;
#pragma unroll
      for (int s = 0; s < 4; ++s) {
        const float wt = __builtin_amdgcn_exp2f(__uint_as_float(g[4 + s].x) - M);
        Ls = fmaf(wt, __uint_as_float(g[4 + s].z), Ls);
        o0 = fmaf(wt, __uint_as_float(g[s].x), o0);
        o1 = fmaf(wt, __uint_as_float(g[s].z), o1);
      }
      *reinterpret_cast<unsigned*>(smem + L_ACT + wave * ACT_STR + lane * 4) = pack2(o0 / Ls, o1 / Ls);
      STAMP(5);
    } else {
      unsigned off[4];
      v4u g[4];
#pragma unroll
      for (int r = 0; r < 4; ++r) off[r] = WS_H + (unsigned)((r * 8 + wave) * 896 + (ckc - 32) * 64 + lane) * 16u;
      get_granules<4>(rs, off, g, tagA, tagB);
      const float s0 = (__uint_as_float(g[0].x) + __uint_as_float(g[1].x)) + (__uint_as_float(g[2].x) + __uint_as_float(g[3].x));
      const float s1 = (__uint_as_float(g[0].z) + __uint_as_float(g[1].z)) + (__uint_as_float(g[2].z) + __uint_as_float(g[3].z));
      const float h0 = bfr(bfr(s0) + bflo(d_b1)), h1 = bfr(bfr(s1) + bfhi(d_b1));
      *reinterpret_cast<unsigned*>(smem + L_ACT + wave * ACT_STR + lane * 4) = pack2(gelu_tanh(h0), gelu_tanh(h1));
    }
    __syncthreads();
    f32x4 acc = {0.f, 0.f, 0.f, 0.f};
#pragma unroll
    for (int ks = 0; ks < 4; ++ks) {
      const v4u bfg = *reinterpret_cast<const v4u*>(smem + L_ACT + (i16 & 7) * ACT_STR + (32 * ks + 8 * g4) * 2);
      acc = mfma16(af[ks], bfg, acc);
    }
    if (i16 < 8) {
      const unsigned po = WS_P + (unsigned)((ckc * 8 + i16) * 256 + cnc * 64 + wave * 8 + g4 * 2) * 16u;
      put_granule(rs, po, acc[0], acc[1], tagA, tagB);
      put_granule(rs, po + 16u, acc[2], acc[3], tagA, tagB);
    }
  }
  STAMP(6);

  {
    const int db = wg >> 5, dg0 = (wg & 31) * 8;
    if (tid < N_KC * 8) {
      const int kc = tid >> 3, gq = tid & 7;
      unsigned off[1] = {WS_P + (unsigned)((kc * 8 + db) * 256 + dg0 + gq) * 16u};
      v4u g[1];
      get_granules<1>(rs, off, g, tagA, tagB);
      *reinterpret_cast<v2u*>(smem + L_PART + kc * 64 + gq * 8) = v2u{g[0].x, g[0].z};
    }
    __syncthreads();
    STAMP(7);
    if (tid < 16) {
      const int n = dg0 * 2 + tid;
      float sa = 0.f, sm = 0.f;
#pragma unroll
      for (int k = 0; k < 32; ++k) sa += *reinterpret_cast<const float*>(smem + L_PART + k * 64 + tid * 4);
#pragma unroll
      for (int k = 32; k < N_KC; ++k) sm += *reinterpret_cast<const float*>(smem + L_PART + k * 64 + tid * 4);
      const float mlp = bfr(bfr(sm) + bf2f(d_b2));
      const float y = bfr(bfr(bf2f(d_x) + bfr(sa)) + mlp);
      out[db * DM + n] = (unsigned short)f2bf(y);
    }
    if (wg == 0 && tid == 0) __hip_atomic_store((gu32*)(ws + WS_E), epoch, __ATOMIC_RELAXED, __HIP_MEMORY_SCOPE_AGENT);
  }
  STAMP_FLUSH;
}

}

extern "C" void kernel_launch(void* const* d_in, const int* in_sizes, int n_in, void* d_out, int out_size,
                              void* d_ws, size_t ws_size, hipStream_t stream) {
  (void)in_sizes; (void)n_in; (void)out_size; (void)ws_size;
  const unsigned short* x = (const unsigned short*)d_in[0];
  const unsigned short* k = (const unsigned short*)d_in[1];
  const unsigned short* v = (const unsigned short*)d_in[2];
  const unsigned short* wq = (const unsigned short*)d_in[3];
  const unsigned short* wo = (const unsigned short*)d_in[4];
  const unsigned short* w1 = (const unsigned short*)d_in[6];
  const unsigned short* b1 = (const unsigned short*)d_in[7];
  const unsigned short* w2 = (const unsigned short*)d_in[8];
  const unsigned short* b2 = (const unsigned short*)d_in[9];
  hipLaunchKernelGGL(fused_decode_kernel, dim3(NWG), dim3(NTHR), 0, stream, x, k, v, wq, wo, w1, b1, w2, b2,
                     (unsigned short*)d_out, (unsigned char*)d_ws);
}
```

```cpp
#include <hip/hip_runtime.h>
#include <hip/hip_bf16.h>
#include <stdint.h>

namespace {

constexpr int DM = 512, NHKV = 8, SK = 4096, DH = 128, WINDOW = 1024, FF = 1792;
constexpr int NWG = 256, NTHR = 640;
#define EXP 0
#define WA_AUX 2
#define QDELAY 0
constexpr int NQC = 4096;
constexpr int N_HWG = FF / 16;
constexpr int N_KC = 32 + FF / 128;
constexpr int N_CWG = N_KC * 4;

typedef unsigned v4u __attribute__((ext_vector_type(4)));
typedef unsigned v2u __attribute__((ext_vector_type(2)));
typedef float f32x4 __attribute__((ext_vector_type(4)));
typedef float f32x2 __attribute__((ext_vector_type(2)));
typedef short bf16x8 __attribute__((ext_vector_type(8)));
typedef short s16x4 __attribute__((ext_vector_type(4)));
typedef __attribute__((address_space(1))) unsigned gu32;
typedef __attribute__((address_space(3))) s16x4 lds_s16x4;
#define LAS __attribute__((address_space(3)))

constexpr unsigned WS_E = 0;
constexpr unsigned WS_Q = 4096;
constexpr unsigned WS_H = WS_Q + 8 * 1024 * 16;
constexpr unsigned WS_O = WS_H + 8 * 448 * 16;
constexpr unsigned WS_ML = WS_O + 32 * 8 * 4 * 64 * 16;
constexpr unsigned WS_P = WS_ML + 32 * 8 * 4 * 16;
constexpr unsigned WS_END = WS_P + N_KC * 8 * 256 * 16;

#define STAMP(i) do {} while (0)
#define STAMP_DECL do {} while (0)
#define STAMP_FLUSH do {} while (0)
constexpr unsigned SPIN_CAP = 1u << 18;

constexpr unsigned L_XN = 0;
constexpr unsigned XN_STR = 1040;
constexpr unsigned L_WA = L_XN + 8 * XN_STR;
constexpr unsigned L_VI = L_WA + 2 * 16384;
constexpr unsigned L_WC = L_VI + 65536;
constexpr unsigned L_P = L_WC + 32768;
constexpr unsigned P_STR = 528;
constexpr unsigned L_MX = L_P + 4 * P_STR;
constexpr unsigned L_LS = L_MX + 128;
constexpr unsigned L_ACT = L_LS + 128;
constexpr unsigned ACT_STR = 272;
constexpr unsigned L_PART = L_ACT + 8 * ACT_STR;
constexpr unsigned L_QS = L_PART + N_KC * 64;
constexpr unsigned L_CNT = L_QS + 1024;
constexpr unsigned L_DBG = L_CNT + 16;
constexpr unsigned L_END = L_DBG + 128;

__device__ __forceinline__ float bf2f(unsigned bits16) { return __builtin_bit_cast(float, bits16 << 16); }
__device__ __forceinline__ float bflo(unsigned w) { return __builtin_bit_cast(float, w << 16); }
__device__ __forceinline__ float bfhi(unsigned w) { return __builtin_bit_cast(float, w & 0xffff0000u); }
__device__ __forceinline__ unsigned f2bf(float f) { return (unsigned)__builtin_bit_cast(unsigned short, (__bf16)f); }
__device__ __forceinline__ float bfr(float f) { return bf2f(f2bf(f)); }
__device__ __forceinline__ unsigned pack2(float lo, float hi) { return f2bf(lo) | (f2bf(hi) << 16); }
template <int CTRL> __device__ __forceinline__ float dpp(float x) {
  return __builtin_bit_cast(float, __builtin_amdgcn_mov_dpp(__builtin_bit_cast(int, x), CTRL, 0xf, 0xf, true));
}
constexpr int XOR1 = 0xB1, ROR1 = 0x121, ROR2 = 0x122, ROR4 = 0x124, ROR8 = 0x128;
__device__ __forceinline__ float row_max(float m) {
  m = fmaxf(m, dpp<ROR1>(m)); m = fmaxf(m, dpp<ROR2>(m)); m = fmaxf(m, dpp<ROR4>(m)); return fmaxf(m, dpp<ROR8>(m));
}
__device__ __forceinline__ float row_sum(float m) {
  m += dpp<ROR1>(m); m += dpp<ROR2>(m); m += dpp<ROR4>(m); return m + dpp<ROR8>(m);
}
__device__ __forceinline__ float wave_sum(float x) {
  x = row_sum(x);
  auto s = __builtin_amdgcn_permlane16_swap(__float_as_uint(x), __float_as_uint(x), false, false);
  x = __uint_as_float(s[0]) + __uint_as_float(s[1]);
  auto t = __builtin_amdgcn_permlane32_swap(__float_as_uint(x), __float_as_uint(x), false, false);
  return __uint_as_float(t[0]) + __uint_as_float(t[1]);
}
__device__ __forceinline__ void wave_lds_sync() {
  __builtin_amdgcn_fence(__ATOMIC_RELEASE, "wavefront");
  __builtin_amdgcn_wave_barrier();
  __builtin_amdgcn_fence(__ATOMIC_ACQUIRE, "wavefront");
}
__device__ __forceinline__ v4u ldnt(const void* p) { return __builtin_nontemporal_load(reinterpret_cast<const v4u*>(p)); }
__device__ __forceinline__ f32x4 mfma16(v4u a, v4u b, f32x4 c) {
  return __builtin_amdgcn_mfma_f32_16x16x32_bf16(__builtin_bit_cast(bf16x8, a), __builtin_bit_cast(bf16x8, b), c, 0, 0, 0);
}
__device__ __forceinline__ unsigned swz(unsigned row, unsigned ch) {
  return 256u * row + 16u * (ch ^ (((row & 3) << 2) | ((row >> 2) & 3)));
}

__device__ __forceinline__ void put_granule(__amdgpu_buffer_rsrc_t rs, unsigned off, unsigned w0, unsigned w1,
                                            unsigned tagA, unsigned tagB) {
  v4u g = {w0, tagA, w1, tagB};
  __builtin_amdgcn_raw_buffer_store_b128(g, rs, (int)off, 0, 16  );
}
template <int N>
__device__ __forceinline__ void load_granules(__amdgpu_buffer_rsrc_t rs, const unsigned (&off)[N], v4u (&g)[N]) {
#pragma unroll
  for (int n = 0; n < N; ++n) {
    unsigned o = off[n];
    asm volatile("" : "+v"(o)::"memory");
    g[n] = __builtin_amdgcn_raw_buffer_load_b128(rs, (int)o, 0, 16  );
  }
}
template <int N>
__device__ __forceinline__ bool granules_ok(const v4u (&g)[N], unsigned tagA, unsigned tagB) {
  bool ok = true;
#pragma unroll
  for (int n = 0; n < N; ++n) ok = ok && (g[n].y == tagA) && (g[n].w == tagB);
  return __all(ok);
}
template <int N>
__device__ __forceinline__ void get_granules(__amdgpu_buffer_rsrc_t rs, const unsigned (&off)[N], v4u (&g)[N],
                                             unsigned tagA, unsigned tagB) {
  for (unsigned spins = 0;; ++spins) {
    asm volatile("" ::: "memory");
    load_granules<N>(rs, off, g);
    if (granules_ok<N>(g, tagA, tagB) || spins > SPIN_CAP) break;
    __builtin_amdgcn_s_sleep(1);
  }
  asm volatile("" ::: "memory");
}

__global__ void __launch_bounds__(NTHR, 3)
fused_decode_kernel(const unsigned short* __restrict__ x, const unsigned short* __restrict__ kc_,
                    const unsigned short* __restrict__ vc_, const unsigned short* __restrict__ wq,
                    const unsigned short* __restrict__ wo, const unsigned short* __restrict__ w1,
                    const unsigned short* __restrict__ b1, const unsigned short* __restrict__ w2,
                    const unsigned short* __restrict__ b2, unsigned short* __restrict__ out,
                    unsigned char* ws) {
  __shared__ __attribute__((aligned(16))) unsigned char smem[L_END];
  const int tid = threadIdx.x, lane = tid & 63;
  const int wave = __builtin_amdgcn_readfirstlane(tid >> 6);
  const int wg = blockIdx.x;
  const int g4 = lane >> 4, i16 = lane & 15;
  const int tq = i16 >> 2, tp = lane & 3;
  const __amdgpu_buffer_rsrc_t rs = __builtin_amdgcn_make_buffer_rsrc((void*)ws, 0, (int)WS_END, 0x00020000);
  const int ab = wg >> 5, kvh = (wg >> 2) & 7, sp = wg & 3;
  STAMP_DECL;
  STAMP(0);

  if (EXP == 4 && wave >= 8) return;
  if (EXP == 7 && wave >= 8) { __builtin_amdgcn_s_barrier(); __builtin_amdgcn_s_barrier(); return; }
  if (wave >= 8) {
    __builtin_amdgcn_s_setprio(3);
    const int sv = wave - 8;
    const int cs = sv == 0 ? ((wg & 7) * 32 + (wg >> 3)) : ((wg & 7) * 14 + (wg >> 3));
    const bool has_w = (sv == 0) || ((wg >> 3) < 14);
    v4u xv[4], wa[16];
    unsigned epoch = __hip_atomic_load((gu32*)(ws + WS_E), __ATOMIC_RELAXED, __HIP_MEMORY_SCOPE_AGENT);
#pragma unroll
    for (int b = 0; b < 4; ++b) xv[b] = *reinterpret_cast<const v4u*>(x + (sv * 4 + b) * DM + lane * 8);
    {
      const __amdgpu_buffer_rsrc_t rsW = __builtin_amdgcn_make_buffer_rsrc(
          (void*)(sv == 0 ? wq : w1), 0, (has_w && EXP != 2) ? (sv == 0 ? DM * NQC * 2 : DM * FF * 2) : 0, 0x00020000);
      const int ldw = sv == 0 ? NQC : FF;
#pragma unroll
      for (int i = 0; i < 16; ++i) {
        const int pi = lane + 64 * i;
        wa[i] = __builtin_amdgcn_raw_buffer_load_b128(rsW, ((pi >> 1) * ldw + cs * 16 + (pi & 1) * 8) * 2, 0, WA_AUX);
      }
    }
    v2u b1v = *reinterpret_cast<const v2u*>(b1 + ((sv == 0 || !has_w) ? 0 : cs * 16 + g4 * 4));
    __builtin_amdgcn_sched_barrier(0);
    __builtin_amdgcn_s_barrier();
    __builtin_amdgcn_sched_barrier(0);
    const unsigned xn = L_XN, img = L_WA + sv * 16384;
#pragma unroll
    for (int b = 0; b < 4; ++b) {
      f32x2 e[4];
#pragma unroll
      for (int w = 0; w < 4; ++w) e[w] = f32x2{bflo(xv[b][w]), bfhi(xv[b][w])};
      const f32x2 s2 = (e[0] + e[1]) + (e[2] + e[3]);
      const float mean = wave_sum(s2.x + s2.y) * (1.0f / DM);
      f32x2 q2 = {0.f, 0.f};
#pragma unroll
      for (int w = 0; w < 4; ++w) { const f32x2 d = e[w] - mean; q2 += d * d; }
      const float var = wave_sum(q2.x + q2.y) * (1.0f / DM);
      const float mb = bfr(mean);
      const float rinv = __builtin_amdgcn_rcpf(bfr(__builtin_amdgcn_sqrtf(bfr(bfr(var) + 1e-5f))));
      v4u o;
#pragma unroll
      for (int w = 0; w < 4; ++w) {
        const f32x2 t = e[w] - mb;
        const unsigned tb = pack2(t.x, t.y);
        const f32x2 u = f32x2{bflo(tb), bfhi(tb)} * rinv;
        o[w] = pack2(u.x, u.y);
      }
      *reinterpret_cast<v4u*>(smem + xn + (sv * 4 + b) * XN_STR + lane * 16) = o;
    }
    STAMP(8);
    {
      __builtin_amdgcn_fence(__ATOMIC_RELEASE, "workgroup");
      LAS unsigned* cnt = (LAS unsigned*)(smem + L_CNT);
      if (lane == 0) __hip_atomic_fetch_add(cnt, 1u, __ATOMIC_RELAXED, __HIP_MEMORY_SCOPE_WORKGROUP);
      if (!has_w) { __builtin_amdgcn_s_barrier(); return; }
#pragma unroll
      for (int i = 0; i < 16; ++i) *reinterpret_cast<v4u*>(smem + img + (lane + 64 * i) * 16) = wa[i];
      while (__hip_atomic_load(cnt, __ATOMIC_RELAXED, __HIP_MEMORY_SCOPE_WORKGROUP) < 2u) __builtin_amdgcn_s_sleep(1);
      __builtin_amdgcn_fence(__ATOMIC_ACQUIRE, "workgroup");
    }
    asm volatile("" : "+v"(epoch), "+v"(b1v));
    epoch += 1u;
    const unsigned tagA = epoch, tagB = epoch ^ 0x9E3779B9u;
    __builtin_amdgcn_sched_barrier(0);
    STAMP(1);
    __builtin_amdgcn_sched_barrier(0);
    wave_lds_sync();
    v4u afr[16], bfr_[16];
#pragma unroll
    for (int ks = 0; ks < 16; ++ks) {
      const s16x4 a0 = __builtin_amdgcn_ds_read_tr16_b64_v4i16((lds_s16x4*)(smem + img + (32 * ks + 8 * g4 + tq) * 32 + tp * 8));
      const s16x4 a1 = __builtin_amdgcn_ds_read_tr16_b64_v4i16((lds_s16x4*)(smem + img + (32 * ks + 8 * g4 + 4 + tq) * 32 + tp * 8));
      const v2u u0 = __builtin_bit_cast(v2u, a0), u1 = __builtin_bit_cast(v2u, a1);
      afr[ks] = v4u{u0.x, u0.y, u1.x, u1.y};
      bfr_[ks] = *reinterpret_cast<const v4u*>(smem + xn + (i16 & 7) * XN_STR + (32 * ks + 8 * g4) * 2);
    }
    __builtin_amdgcn_sched_barrier(0);
    f32x4 ac4[4] = {{0.f, 0.f, 0.f, 0.f}, {0.f, 0.f, 0.f, 0.f}, {0.f, 0.f, 0.f, 0.f}, {0.f, 0.f, 0.f, 0.f}};
#pragma unroll
    for (int ks = 0; ks < 16; ++ks) ac4[ks & 3] = mfma16(afr[ks], bfr_[ks], ac4[ks & 3]);
    const f32x4 acc = (ac4[0] + ac4[1]) + (ac4[2] + ac4[3]);
    if (i16 < 8) {
      if (sv == 0) {
        put_granule(rs, WS_Q + (unsigned)(i16 * 1024 + cs * 4 + g4) * 16u, pack2(acc[0], acc[1]), pack2(acc[2], acc[3]), tagA, tagB);
      } else {
        float hv[4];
#pragma unroll
        for (int j = 0; j < 4; ++j) {
          const float bj = (j & 1) ? bfhi(b1v[j >> 1]) : bflo(b1v[j >> 1]);
          const float h = bfr(bfr(acc[j]) + bj);
          const float u = 0.7978845608028654f * (h + 0.044715f * h * h * h);
          hv[j] = 0.5f * h * (1.0f + tanhf(u));
        }
        put_granule(rs, WS_H + (unsigned)(i16 * 448 + cs * 4 + g4) * 16u, pack2(hv[0], hv[1]), pack2(hv[2], hv[3]), tagA, tagB);
      }
    }
    STAMP(2);
    if (sv == 0) {
      __builtin_amdgcn_s_sleep(QDELAY);
      unsigned qoff[2];
      v4u qg[2];
#pragma unroll
      for (int e = 0; e < 2; ++e) qoff[e] = WS_Q + (unsigned)(ab * 1024 + kvh * 128 + lane + 64 * e) * 16u;
      get_granules<2>(rs, qoff, qg, tagA, tagB);
#pragma unroll
      for (int e = 0; e < 2; ++e) *reinterpret_cast<v2u*>(smem + L_QS + (lane + 64 * e) * 8) = v2u{qg[e].x, qg[e].z};
      STAMP(9);
    }
    __syncthreads();
    return;
  }

  const int ckc = wg >> 2, cnc = wg & 3;
  const int d_n = (wg & 31) * 16 + i16;
  unsigned epoch = __hip_atomic_load((gu32*)(ws + WS_E), __ATOMIC_RELAXED, __HIP_MEMORY_SCOPE_AGENT);
  const unsigned d_b2 = b2[d_n];
  const unsigned d_x = x[(wg >> 5) * DM + d_n];
  if (tid == 0) __hip_atomic_store((LAS unsigned*)(smem + L_CNT), 0u, __ATOMIC_RELAXED, __HIP_MEMORY_SCOPE_WORKGROUP);
  __builtin_amdgcn_sched_barrier(0);
  asm volatile("s_waitcnt lgkmcnt(0)" ::: "memory");
  __builtin_amdgcn_s_barrier();
  __builtin_amdgcn_sched_barrier(0);
  v4u Kr[8], Vr[8], Wc[4];
  {
    const size_t kv0 = ((size_t)(ab * NHKV + kvh) * SK + (SK - WINDOW) + sp * 256 + wave * 32) * DH;
#pragma unroll
    for (int t = 0; t < 2; ++t)
#pragma unroll
      for (int kk = 0; kk < 4; ++kk) Kr[t * 4 + kk] = ldnt(kc_ + kv0 + (size_t)(16 * t + i16) * DH + 32 * kk + 8 * g4);
#pragma unroll
    for (int i = 0; i < 8; ++i) Vr[i] = ldnt(vc_ + kv0 + (size_t)(4 * i + g4) * DH + i16 * 8);
    const unsigned short* wb = (ckc < 32) ? (wo + (size_t)ckc * 128 * DM) : (w2 + (size_t)(ckc - 32) * 128 * DM);
    const __amdgpu_buffer_rsrc_t rsC =
        __builtin_amdgcn_make_buffer_rsrc((void*)wb, 0, (wg < N_CWG && EXP != 3) ? 128 * DM * 2 : 0, 0x00020000);
#pragma unroll
    for (int i = 0; i < 4; ++i)
      Wc[i] = __builtin_amdgcn_raw_buffer_load_b128(rsC, (((tid >> 4) + 32 * i) * DM + cnc * 128 + i16 * 8) * 2, 0, 2);
  }
  __builtin_amdgcn_sched_barrier(0);
  STAMP(1);
  __syncthreads();
  STAMP(2);
  asm volatile("" : "+v"(epoch));
  epoch += 1u;
  const unsigned tagA = epoch, tagB = epoch ^ 0x9E3779B9u;

  {
    f32x4 sc[2];
#pragma unroll
    for (int t = 0; t < 2; ++t) {
      f32x4 a = {0.f, 0.f, 0.f, 0.f};
#pragma unroll
      for (int kk = 0; kk < 4; ++kk) {
        const v4u qa = *reinterpret_cast<const v4u*>(smem + L_QS + ((i16 & 3) * 128 + 32 * kk + 8 * g4) * 2);
        a = mfma16(qa, Kr[t * 4 + kk], a);
      }
#pragma unroll
      for (int h = 0; h < 4; ++h) sc[t][h] = bfr(bfr(a[h]) * 0.08838834764831845f) * 1.4426950408889634f;
    }
    if (g4 == 0) {
      f32x4 mw;
#pragma unroll
      for (int h = 0; h < 4; ++h) mw[h] = row_max(fmaxf(sc[0][h], sc[1][h]));
      if (i16 == 0) *reinterpret_cast<f32x4*>(smem + L_MX + wave * 16) = mw;
    }
    STAMP(3);
    __syncthreads();
    __builtin_amdgcn_sched_barrier(0);
    STAMP(8);
    __builtin_amdgcn_sched_barrier(0);
    f32x4 M = *reinterpret_cast<const f32x4*>(smem + L_MX);
#pragma unroll
    for (int w = 1; w < 8; ++w) {
      const f32x4 m2 = *reinterpret_cast<const f32x4*>(smem + L_MX + w * 16);
#pragma unroll
      for (int h = 0; h < 4; ++h) M[h] = fmaxf(M[h], m2[h]);
    }
    if (g4 == 0) {
      f32x4 lw;
#pragma unroll
      for (int h = 0; h < 4; ++h) {
        float l = 0.f;
#pragma unroll
        for (int t = 0; t < 2; ++t) {
          const unsigned pb = f2bf(__builtin_amdgcn_exp2f(sc[t][h] - M[h]));
          *reinterpret_cast<unsigned short*>(smem + L_P + h * P_STR + (wave * 32 + t * 16 + i16) * 2) = (unsigned short)pb;
          l += bf2f(pb);
        }
        lw[h] = row_sum(l);
      }
      if (i16 == 0) *reinterpret_cast<f32x4*>(smem + L_LS + wave * 16) = lw;
    }
    __builtin_amdgcn_sched_barrier(0);
    STAMP(9);
    __builtin_amdgcn_sched_barrier(0);
#pragma unroll
    for (int i = 0; i < 8; ++i)
      *reinterpret_cast<v4u*>(smem + L_VI + swz(wave * 32 + 4 * i + g4, i16)) = Vr[i];
    STAMP(10);
    __syncthreads();
    v4u pa[8], vb[8];
#pragma unroll
    for (int ks = 0; ks < 8; ++ks) {
      pa[ks] = *reinterpret_cast<const v4u*>(smem + L_P + (i16 & 3) * P_STR + (32 * ks + 8 * g4) * 2);
      const s16x4 b0 = __builtin_amdgcn_ds_read_tr16_b64_v4i16(
          (lds_s16x4*)(smem + L_VI + swz(32 * ks + 8 * g4 + tq, 2 * wave + (tp >> 1)) + 8 * (tp & 1)));
      const s16x4 b1_ = __builtin_amdgcn_ds_read_tr16_b64_v4i16(
          (lds_s16x4*)(smem + L_VI + swz(32 * ks + 8 * g4 + 4 + tq, 2 * wave + (tp >> 1)) + 8 * (tp & 1)));
      const v2u u0 = __builtin_bit_cast(v2u, b0), u1 = __builtin_bit_cast(v2u, b1_);
      vb[ks] = v4u{u0.x, u0.y, u1.x, u1.y};
    }
    __builtin_amdgcn_sched_barrier(0);
    f32x4 o2[2] = {{0.f, 0.f, 0.f, 0.f}, {0.f, 0.f, 0.f, 0.f}};
#pragma unroll
    for (int ks = 0; ks < 8; ++ks) o2[ks & 1] = mfma16(pa[ks], vb[ks], o2[ks & 1]);
    const f32x4 o = o2[0] + o2[1];
    const float og = g4 == 0 ? o[0] : g4 == 1 ? o[1] : g4 == 2 ? o[2] : o[3];
    const float on = dpp<XOR1>(og);
    const unsigned slot = (unsigned)(((kvh * 4 + g4) * 8 + ab) * 4 + sp);
    if ((i16 & 1) == 0)
      put_granule(rs, WS_O + (slot * 64u + (unsigned)(wave * 8 + (i16 >> 1))) * 16u, __float_as_uint(og), __float_as_uint(on), tagA, tagB);
    if (wave == 0 && i16 == 0) {
      float Ls = 0.f;
#pragma unroll
      for (int w = 0; w < 8; ++w) Ls += *reinterpret_cast<const float*>(smem + L_LS + w * 16 + g4 * 4);
      const float Mg = g4 == 0 ? M[0] : g4 == 1 ? M[1] : g4 == 2 ? M[2] : M[3];
      put_granule(rs, WS_ML + slot * 16u, __float_as_uint(Mg), __float_as_uint(Ls), tagA, tagB);
    }
  }
  STAMP(4);

  if (wg < N_CWG) {
#pragma unroll
    for (int i = 0; i < 4; ++i) *reinterpret_cast<v4u*>(smem + L_WC + swz((tid >> 4) + 32 * i, i16)) = Wc[i];
    __syncthreads();
    v4u af[4];
#pragma unroll
    for (int ks = 0; ks < 4; ++ks) {
      const s16x4 a0 = __builtin_amdgcn_ds_read_tr16_b64_v4i16(
          (lds_s16x4*)(smem + L_WC + swz(32 * ks + 8 * g4 + tq, 2 * wave + (tp >> 1)) + 8 * (tp & 1)));
      const s16x4 a1 = __builtin_amdgcn_ds_read_tr16_b64_v4i16(
          (lds_s16x4*)(smem + L_WC + swz(32 * ks + 8 * g4 + 4 + tq, 2 * wave + (tp >> 1)) + 8 * (tp & 1)));
      const v2u u0 = __builtin_bit_cast(v2u, a0), u1 = __builtin_bit_cast(v2u, a1);
      af[ks] = v4u{u0.x, u0.y, u1.x, u1.y};
    }
    if (ckc < 32) {
      unsigned off[8];
      v4u g[8];
#pragma unroll
      for (int s = 0; s < 4; ++s) {
        const unsigned slot = (unsigned)((ckc * 8 + wave) * 4 + s);
        off[s] = WS_O + (slot * 64u + lane) * 16u;
        off[4 + s] = WS_ML + slot * 16u;
      }
      get_granules<8>(rs, off, g, tagA, tagB);
      float M = __uint_as_float(g[4].x);
#pragma unroll
      for (int s = 1; s < 4; ++s) M = fmaxf(M, __uint_as_float(g[4 + s].x));
      float Ls = 0.f, o0 = 0.f, o1 = 0.f;
#pragma unroll
      for (int s = 0; s < 4; ++s) {
        const float wt = __builtin_amdgcn_exp2f(__uint_as_float(g[4 + s].x) - M);
        Ls = fmaf(wt, __uint_as_float(g[4 + s].z), Ls);
        o0 = fmaf(wt, __uint_as_float(g[s].x), o0);
        o1 = fmaf(wt, __uint_as_float(g[s].z), o1);
      }
      *reinterpret_cast<unsigned*>(smem + L_ACT + wave * ACT_STR + lane * 4) = pack2(o0 / Ls, o1 / Ls);
      STAMP(5);
    } else if (lane < 32) {
      unsigned off[1] = {WS_H + (unsigned)(wave * 448 + (ckc - 32) * 32 + lane) * 16u};
      v4u g[1];
      get_granules<1>(rs, off, g, tagA, tagB);
      *reinterpret_cast<v2u*>(smem + L_ACT + wave * ACT_STR + lane * 8) = v2u{g[0].x, g[0].z};
    }
    __syncthreads();
    f32x4 acc = {0.f, 0.f, 0.f, 0.f};
#pragma unroll
    for (int ks = 0; ks < 4; ++ks) {
      const v4u bfg = *reinterpret_cast<const v4u*>(smem + L_ACT + (i16 & 7) * ACT_STR + (32 * ks + 8 * g4) * 2);
      acc = mfma16(af[ks], bfg, acc);
    }
    if (i16 < 8) {
      const unsigned po = WS_P + (unsigned)((ckc * 8 + i16) * 256 + cnc * 64 + wave * 8 + g4 * 2) * 16u;
      put_granule(rs, po, __float_as_uint(acc[0]), __float_as_uint(acc[1]), tagA, tagB);
      put_granule(rs, po + 16u, __float_as_uint(acc[2]), __float_as_uint(acc[3]), tagA, tagB);
    }
  }
  STAMP(6);

  {
    const int db = wg >> 5, dg0 = (wg & 31) * 8;
    if (tid < N_KC * 8) {
      const int kc = tid >> 3, gq = tid & 7;
      unsigned off[1] = {WS_P + (unsigned)((kc * 8 + db) * 256 + dg0 + gq) * 16u};
      v4u g[1];
      get_granules<1>(rs, off, g, tagA, tagB);
      *reinterpret_cast<v2u*>(smem + L_PART + kc * 64 + gq * 8) = v2u{g[0].x, g[0].z};
    }
    __syncthreads();
    STAMP(7);
    if (tid < 16) {
      const int n = dg0 * 2 + tid;
      float sa = 0.f, sm = 0.f;
#pragma unroll
      for (int k = 0; k < 32; ++k) sa += *reinterpret_cast<const float*>(smem + L_PART + k * 64 + tid * 4);
#pragma unroll
      for (int k = 32; k < N_KC; ++k) sm += *reinterpret_cast<const float*>(smem + L_PART + k * 64 + tid * 4);
      const float mlp = bfr(bfr(sm) + bf2f(d_b2));
      const float y = bfr(bfr(bf2f(d_x) + bfr(sa)) + mlp);
      out[db * DM + n] = (unsigned short)f2bf(y);
    }
    if (wg == 0 && tid == 0) __hip_atomic_store((gu32*)(ws + WS_E), epoch, __ATOMIC_RELAXED, __HIP_MEMORY_SCOPE_AGENT);
  }
  STAMP_FLUSH;
}

}

extern "C" void kernel_launch(void* const* d_in, const int* in_sizes, int n_in, void* d_out, int out_size,
                              void* d_ws, size_t ws_size, hipStream_t stream) {
  (void)in_sizes; (void)n_in; (void)out_size; (void)ws_size;
  const unsigned short* x = (const unsigned short*)d_in[0];
  const unsigned short* k = (const unsigned short*)d_in[1];
  const unsigned short* v = (const unsigned short*)d_in[2];
  const unsigned short* wq = (const unsigned short*)d_in[3];
  const unsigned short* wo = (const unsigned short*)d_in[4];
  const unsigned short* w1 = (const unsigned short*)d_in[6];
  const unsigned short* b1 = (const unsigned short*)d_in[7];
  const unsigned short* w2 = (const unsigned short*)d_in[8];
  const unsigned short* b2 = (const unsigned short*)d_in[9];
  hipLaunchKernelGGL(fused_decode_kernel, dim3(NWG), dim3(NTHR), 0, stream, x, k, v, wq, wo, w1, b1, w2, b2,
                     (unsigned short*)d_out, (unsigned char*)d_ws);
}
```

```cpp
#include <hip/hip_runtime.h>
#include <hip/hip_bf16.h>
#include <stdint.h>

namespace {

constexpr int DM = 512, NHKV = 8, SK = 4096, DH = 128, WINDOW = 1024, FF = 1792;
constexpr int NWG = 256, NTHR = 640;
#define EXP 0
#define WA_AUX 2
#define QDELAY 8
constexpr int NQC = 4096;
constexpr int N_HWG = FF / 16;
constexpr int N_KC = 32 + FF / 128;
constexpr int N_CWG = N_KC * 4;

typedef unsigned v4u __attribute__((ext_vector_type(4)));
typedef unsigned v2u __attribute__((ext_vector_type(2)));
typedef float f32x4 __attribute__((ext_vector_type(4)));
typedef float f32x2 __attribute__((ext_vector_type(2)));
typedef short bf16x8 __attribute__((ext_vector_type(8)));
typedef short s16x4 __attribute__((ext_vector_type(4)));
typedef __attribute__((address_space(1))) unsigned gu32;
typedef __attribute__((address_space(3))) s16x4 lds_s16x4;
#define LAS __attribute__((address_space(3)))

constexpr unsigned WS_E = 0;
constexpr unsigned WS_Q = 4096;
constexpr unsigned WS_H = WS_Q + 8 * 1024 * 16;
constexpr unsigned WS_O = WS_H + 8 * 448 * 16;
constexpr unsigned WS_ML = WS_O + 32 * 8 * 4 * 64 * 16;
constexpr unsigned WS_P = WS_ML + 32 * 8 * 4 * 16;
constexpr unsigned WS_END = WS_P + N_KC * 8 * 256 * 16;

#define STAMP(i) do {} while (0)
#define STAMP_DECL do {} while (0)
#define STAMP_FLUSH do {} while (0)
constexpr unsigned SPIN_CAP = 1u << 18;

constexpr unsigned L_XN = 0;
constexpr unsigned XN_STR = 1040;
constexpr unsigned L_WA = L_XN + 8 * XN_STR;
constexpr unsigned L_VI = L_WA + 2 * 16384;
constexpr unsigned L_WC = L_VI + 65536;
constexpr unsigned L_P = L_WC + 32768;
constexpr unsigned P_STR = 528;
constexpr unsigned L_MX = L_P + 4 * P_STR;
constexpr unsigned L_LS = L_MX + 128;
constexpr unsigned L_ACT = L_LS + 128;
constexpr unsigned ACT_STR = 272;
constexpr unsigned L_PART = L_ACT + 8 * ACT_STR;
constexpr unsigned L_QS = L_PART + N_KC * 64;
constexpr unsigned L_CNT = L_QS + 1024;
constexpr unsigned L_DBG = L_CNT + 16;
constexpr unsigned L_END = L_DBG + 128;

__device__ __forceinline__ float bf2f(unsigned bits16) { return __builtin_bit_cast(float, bits16 << 16); }
__device__ __forceinline__ float bflo(unsigned w) { return __builtin_bit_cast(float, w << 16); }
__device__ __forceinline__ float bfhi(unsigned w) { return __builtin_bit_cast(float, w & 0xffff0000u); }
__device__ __forceinline__ unsigned f2bf(float f) { return (unsigned)__builtin_bit_cast(unsigned short, (__bf16)f); }
__device__ __forceinline__ float bfr(float f) { return bf2f(f2bf(f)); }
__device__ __forceinline__ unsigned pack2(float lo, float hi) { return f2bf(lo) | (f2bf(hi) << 16); }
template <int CTRL> __device__ __forceinline__ float dpp(float x) {
  return __builtin_bit_cast(float, __builtin_amdgcn_mov_dpp(__builtin_bit_cast(int, x), CTRL, 0xf, 0xf, true));
}
constexpr int XOR1 = 0xB1, ROR1 = 0x121, ROR2 = 0x122, ROR4 = 0x124, ROR8 = 0x128;
__device__ __forceinline__ float row_max(float m) {
  m = fmaxf(m, dpp<ROR1>(m)); m = fmaxf(m, dpp<ROR2>(m)); m = fmaxf(m, dpp<ROR4>(m)); return fmaxf(m, dpp<ROR8>(m));
}
__device__ __forceinline__ float row_sum(float m) {
  m += dpp<ROR1>(m); m += dpp<ROR2>(m); m += dpp<ROR4>(m); return m + dpp<ROR8>(m);
}
__device__ __forceinline__ float wave_sum(float x) {
  x = row_sum(x);
  auto s = __builtin_amdgcn_permlane16_swap(__float_as_uint(x), __float_as_uint(x), false, false);
  x = __uint_as_float(s[0]) + __uint_as_float(s[1]);
  auto t = __builtin_amdgcn_permlane32_swap(__float_as_uint(x), __float_as_uint(x), false, false);
  return __uint_as_float(t[0]) + __uint_as_float(t[1]);
}
__device__ __forceinline__ void wave_lds_sync() {
  __builtin_amdgcn_fence(__ATOMIC_RELEASE, "wavefront");
  __builtin_amdgcn_wave_barrier();
  __builtin_amdgcn_fence(__ATOMIC_ACQUIRE, "wavefront");
}
__device__ __forceinline__ v4u ldnt(const void* p) { return __builtin_nontemporal_load(reinterpret_cast<const v4u*>(p)); }
__device__ __forceinline__ f32x4 mfma16(v4u a, v4u b, f32x4 c) {
  return __builtin_amdgcn_mfma_f32_16x16x32_bf16(__builtin_bit_cast(bf16x8, a), __builtin_bit_cast(bf16x8, b), c, 0, 0, 0);
}
__device__ __forceinline__ unsigned swz(unsigned row, unsigned ch) {
  return 256u * row + 16u * (ch ^ (((row & 3) << 2) | ((row >> 2) & 3)));
}

__device__ __forceinline__ void put_granule(__amdgpu_buffer_rsrc_t rs, unsigned off, unsigned w0, unsigned w1,
                                            unsigned tagA, unsigned tagB) {
  v4u g = {w0, tagA, w1, tagB};
  __builtin_amdgcn_raw_buffer_store_b128(g, rs, (int)off, 0, 16  );
}
template <int N>
__device__ __forceinline__ void load_granules(__amdgpu_buffer_rsrc_t rs, const unsigned (&off)[N], v4u (&g)[N]) {
#pragma unroll
  for (int n = 0; n < N; ++n) {
    unsigned o = off[n];
    asm volatile("" : "+v"(o)::"memory");
    g[n] = __builtin_amdgcn_raw_buffer_load_b128(rs, (int)o, 0, 16  );
  }
}
template <int N>
__device__ __forceinline__ bool granules_ok(const v4u (&g)[N], unsigned tagA, unsigned tagB) {
  bool ok = true;
#pragma unroll
  for (int n = 0; n < N; ++n) ok = ok && (g[n].y == tagA) && (g[n].w == tagB);
  return __all(ok);
}
template <int N, int SLP = 1>
__device__ __forceinline__ void get_granules(__amdgpu_buffer_rsrc_t rs, const unsigned (&off)[N], v4u (&g)[N],
                                             unsigned tagA, unsigned tagB) {
  for (unsigned spins = 0;; ++spins) {
    asm volatile("" ::: "memory");
    load_granules<N>(rs, off, g);
    if (granules_ok<N>(g, tagA, tagB) || spins > SPIN_CAP) break;
    __builtin_amdgcn_s_sleep(SLP);
  }
  asm volatile("" ::: "memory");
}

__global__ void __launch_bounds__(NTHR, 3)
fused_decode_kernel(const unsigned short* __restrict__ x, const unsigned short* __restrict__ kc_,
                    const unsigned short* __restrict__ vc_, const unsigned short* __restrict__ wq,
                    const unsigned short* __restrict__ wo, const unsigned short* __restrict__ w1,
                    const unsigned short* __restrict__ b1, const unsigned short* __restrict__ w2,
                    const unsigned short* __restrict__ b2, unsigned short* __restrict__ out,
                    unsigned char* ws) {
  __shared__ __attribute__((aligned(16))) unsigned char smem[L_END];
  const int tid = threadIdx.x, lane = tid & 63;
  const int wave = __builtin_amdgcn_readfirstlane(tid >> 6);
  const int wg = blockIdx.x;
  const int g4 = lane >> 4, i16 = lane & 15;
  const int tq = i16 >> 2, tp = lane & 3;
  const __amdgpu_buffer_rsrc_t rs = __builtin_amdgcn_make_buffer_rsrc((void*)ws, 0, (int)WS_END, 0x00020000);
  const int ab = wg >> 5, kvh = (wg >> 2) & 7, sp = wg & 3;
  STAMP_DECL;
  STAMP(0);

  if (EXP == 4 && wave >= 8) return;
  if (EXP == 7 && wave >= 8) { __builtin_amdgcn_s_barrier(); __builtin_amdgcn_s_barrier(); return; }
  if (wave >= 8) {
    __builtin_amdgcn_s_setprio(3);
    const int sv = wave - 8;
    const int cs = sv == 0 ? ((wg & 7) * 32 + (wg >> 3)) : ((wg & 7) * 14 + (wg >> 3));
    const bool has_w = (sv == 0) || ((wg >> 3) < 14);
    v4u xv[4], wa[16];
    unsigned epoch = __hip_atomic_load((gu32*)(ws + WS_E), __ATOMIC_RELAXED, __HIP_MEMORY_SCOPE_AGENT);
#pragma unroll
    for (int b = 0; b < 4; ++b) xv[b] = *reinterpret_cast<const v4u*>(x + (sv * 4 + b) * DM + lane * 8);
    {
      const __amdgpu_buffer_rsrc_t rsW = __builtin_amdgcn_make_buffer_rsrc(
          (void*)(sv == 0 ? wq : w1), 0, (has_w && EXP != 2) ? (sv == 0 ? DM * NQC * 2 : DM * FF * 2) : 0, 0x00020000);
      const int ldw = sv == 0 ? NQC : FF;
#pragma unroll
      for (int i = 0; i < 16; ++i) {
        const int pi = lane + 64 * i;
        wa[i] = __builtin_amdgcn_raw_buffer_load_b128(rsW, ((pi >> 1) * ldw + cs * 16 + (pi & 1) * 8) * 2, 0, WA_AUX);
      }
    }
    v2u b1v = *reinterpret_cast<const v2u*>(b1 + ((sv == 0 || !has_w) ? 0 : cs * 16 + g4 * 4));
    __builtin_amdgcn_sched_barrier(0);
    __builtin_amdgcn_s_barrier();
    __builtin_amdgcn_sched_barrier(0);
    const unsigned xn = L_XN, img = L_WA + sv * 16384;
#pragma unroll
    for (int b = 0; b < 4; ++b) {
      f32x2 e[4];
#pragma unroll
      for (int w = 0; w < 4; ++w) e[w] = f32x2{bflo(xv[b][w]), bfhi(xv[b][w])};
      const f32x2 s2 = (e[0] + e[1]) + (e[2] + e[3]);
      const float mean = wave_sum(s2.x + s2.y) * (1.0f / DM);
      f32x2 q2 = {0.f, 0.f};
#pragma unroll
      for (int w = 0; w < 4; ++w) { const f32x2 d = e[w] - mean; q2 += d * d; }
      const float var = wave_sum(q2.x + q2.y) * (1.0f / DM);
      const float mb = bfr(mean);
      const float rinv = __builtin_amdgcn_rcpf(bfr(__builtin_amdgcn_sqrtf(bfr(bfr(var) + 1e-5f))));
      v4u o;
#pragma unroll
      for (int w = 0; w < 4; ++w) {
        const f32x2 t = e[w] - mb;
        const unsigned tb = pack2(t.x, t.y);
        const f32x2 u = f32x2{bflo(tb), bfhi(tb)} * rinv;
        o[w] = pack2(u.x, u.y);
      }
      *reinterpret_cast<v4u*>(smem + xn + (sv * 4 + b) * XN_STR + lane * 16) = o;
    }
    STAMP(8);
    {
      __builtin_amdgcn_fence(__ATOMIC_RELEASE, "workgroup");
      LAS unsigned* cnt = (LAS unsigned*)(smem + L_CNT);
      if (lane == 0) __hip_atomic_fetch_add(cnt, 1u, __ATOMIC_RELAXED, __HIP_MEMORY_SCOPE_WORKGROUP);
      if (!has_w) { __builtin_amdgcn_s_barrier(); return; }
#pragma unroll
      for (int i = 0; i < 16; ++i) *reinterpret_cast<v4u*>(smem + img + (lane + 64 * i) * 16) = wa[i];
      while (__hip_atomic_load(cnt, __ATOMIC_RELAXED, __HIP_MEMORY_SCOPE_WORKGROUP) < 2u) __builtin_amdgcn_s_sleep(1);
      __builtin_amdgcn_fence(__ATOMIC_ACQUIRE, "workgroup");
    }
    asm volatile("" : "+v"(epoch), "+v"(b1v));
    epoch += 1u;
    const unsigned tagA = epoch, tagB = epoch ^ 0x9E3779B9u;
    __builtin_amdgcn_sched_barrier(0);
    STAMP(1);
    __builtin_amdgcn_sched_barrier(0);
    wave_lds_sync();
    v4u afr[16], bfr_[16];
#pragma unroll
    for (int ks = 0; ks < 16; ++ks) {
      const s16x4 a0 = __builtin_amdgcn_ds_read_tr16_b64_v4i16((lds_s16x4*)(smem + img + (32 * ks + 8 * g4 + tq) * 32 + tp * 8));
      const s16x4 a1 = __builtin_amdgcn_ds_read_tr16_b64_v4i16((lds_s16x4*)(smem + img + (32 * ks + 8 * g4 + 4 + tq) * 32 + tp * 8));
      const v2u u0 = __builtin_bit_cast(v2u, a0), u1 = __builtin_bit_cast(v2u, a1);
      afr[ks] = v4u{u0.x, u0.y, u1.x, u1.y};
      bfr_[ks] = *reinterpret_cast<const v4u*>(smem + xn + (i16 & 7) * XN_STR + (32 * ks + 8 * g4) * 2);
    }
    __builtin_amdgcn_sched_barrier(0);
    f32x4 ac4[4] = {{0.f, 0.f, 0.f, 0.f}, {0.f, 0.f, 0.f, 0.f}, {0.f, 0.f, 0.f, 0.f}, {0.f, 0.f, 0.f, 0.f}};
#pragma unroll
    for (int ks = 0; ks < 16; ++ks) ac4[ks & 3] = mfma16(afr[ks], bfr_[ks], ac4[ks & 3]);
    const f32x4 acc = (ac4[0] + ac4[1]) + (ac4[2] + ac4[3]);
    if (i16 < 8) {
      if (sv == 0) {
        put_granule(rs, WS_Q + (unsigned)(i16 * 1024 + cs * 4 + g4) * 16u, pack2(acc[0], acc[1]), pack2(acc[2], acc[3]), tagA, tagB);
      } else {
        float hv[4];
#pragma unroll
        for (int j = 0; j < 4; ++j) {
          const float bj = (j & 1) ? bfhi(b1v[j >> 1]) : bflo(b1v[j >> 1]);
          const float h = bfr(bfr(acc[j]) + bj);
          const float u = 0.7978845608028654f * (h + 0.044715f * h * h * h);
          hv[j] = 0.5f * h * (1.0f + tanhf(u));
        }
        put_granule(rs, WS_H + (unsigned)(i16 * 448 + cs * 4 + g4) * 16u, pack2(hv[0], hv[1]), pack2(hv[2], hv[3]), tagA, tagB);
      }
    }
    STAMP(2);
    if (sv == 0) {
      __builtin_amdgcn_s_sleep(QDELAY);
      unsigned qoff[2];
      v4u qg[2];
#pragma unroll
      for (int e = 0; e < 2; ++e) qoff[e] = WS_Q + (unsigned)(ab * 1024 + kvh * 128 + lane + 64 * e) * 16u;
      get_granules<2>(rs, qoff, qg, tagA, tagB);
#pragma unroll
      for (int e = 0; e < 2; ++e) *reinterpret_cast<v2u*>(smem + L_QS + (lane + 64 * e) * 8) = v2u{qg[e].x, qg[e].z};
      STAMP(9);
    }
    __syncthreads();
    return;
  }

  const int ckc = wg >> 2, cnc = wg & 3;
  const int d_n = (wg & 31) * 16 + i16;
  unsigned epoch = __hip_atomic_load((gu32*)(ws + WS_E), __ATOMIC_RELAXED, __HIP_MEMORY_SCOPE_AGENT);
  const unsigned d_b2 = b2[d_n];
  const unsigned d_x = x[(wg >> 5) * DM + d_n];
  if (tid == 0) __hip_atomic_store((LAS unsigned*)(smem + L_CNT), 0u, __ATOMIC_RELAXED, __HIP_MEMORY_SCOPE_WORKGROUP);
  __builtin_amdgcn_sched_barrier(0);
  asm volatile("s_waitcnt lgkmcnt(0)" ::: "memory");
  __builtin_amdgcn_s_barrier();
  __builtin_amdgcn_sched_barrier(0);
  v4u Kr[8], Vr[8], Wc[4];
  {
    const size_t kv0 = ((size_t)(ab * NHKV + kvh) * SK + (SK - WINDOW) + sp * 256 + wave * 32) * DH;
#pragma unroll
    for (int t = 0; t < 2; ++t)
#pragma unroll
      for (int kk = 0; kk < 4; ++kk) Kr[t * 4 + kk] = ldnt(kc_ + kv0 + (size_t)(16 * t + i16) * DH + 32 * kk + 8 * g4);
#pragma unroll
    for (int i = 0; i < 8; ++i) Vr[i] = ldnt(vc_ + kv0 + (size_t)(4 * i + g4) * DH + i16 * 8);
    const unsigned short* wb = (ckc < 32) ? (wo + (size_t)ckc * 128 * DM) : (w2 + (size_t)(ckc - 32) * 128 * DM);
    const __amdgpu_buffer_rsrc_t rsC =
        __builtin_amdgcn_make_buffer_rsrc((void*)wb, 0, (wg < N_CWG && EXP != 3) ? 128 * DM * 2 : 0, 0x00020000);
#pragma unroll
    for (int i = 0; i < 4; ++i)
      Wc[i] = __builtin_amdgcn_raw_buffer_load_b128(rsC, (((tid >> 4) + 32 * i) * DM + cnc * 128 + i16 * 8) * 2, 0, 2);
  }
  __builtin_amdgcn_sched_barrier(0);
  STAMP(1);
  __syncthreads();
  STAMP(2);
  asm volatile("" : "+v"(epoch));
  epoch += 1u;
  const unsigned tagA = epoch, tagB = epoch ^ 0x9E3779B9u;

  {
    f32x4 sc[2];
#pragma unroll
    for (int t = 0; t < 2; ++t) {
      f32x4 a = {0.f, 0.f, 0.f, 0.f};
#pragma unroll
      for (int kk = 0; kk < 4; ++kk) {
        const v4u qa = *reinterpret_cast<const v4u*>(smem + L_QS + ((i16 & 3) * 128 + 32 * kk + 8 * g4) * 2);
        a = mfma16(qa, Kr[t * 4 + kk], a);
      }
#pragma unroll
      for (int h = 0; h < 4; ++h) sc[t][h] = bfr(bfr(a[h]) * 0.08838834764831845f) * 1.4426950408889634f;
    }
    if (g4 == 0) {
      f32x4 mw;
#pragma unroll
      for (int h = 0; h < 4; ++h) mw[h] = row_max(fmaxf(sc[0][h], sc[1][h]));
      if (i16 == 0) *reinterpret_cast<f32x4*>(smem + L_MX + wave * 16) = mw;
    }
    STAMP(3);
    __syncthreads();
    __builtin_amdgcn_sched_barrier(0);
    STAMP(8);
    __builtin_amdgcn_sched_barrier(0);
    f32x4 M = *reinterpret_cast<const f32x4*>(smem + L_MX);
#pragma unroll
    for (int w = 1; w < 8; ++w) {
      const f32x4 m2 = *reinterpret_cast<const f32x4*>(smem + L_MX + w * 16);
#pragma unroll
      for (int h = 0; h < 4; ++h) M[h] = fmaxf(M[h], m2[h]);
    }
    if (g4 == 0) {
      f32x4 lw;
#pragma unroll
      for (int h = 0; h < 4; ++h) {
        float l = 0.f;
#pragma unroll
        for (int t = 0; t < 2; ++t) {
          const unsigned pb = f2bf(__builtin_amdgcn_exp2f(sc[t][h] - M[h]));
          *reinterpret_cast<unsigned short*>(smem + L_P + h * P_STR + (wave * 32 + t * 16 + i16) * 2) = (unsigned short)pb;
          l += bf2f(pb);
        }
        lw[h] = row_sum(l);
      }
      if (i16 == 0) *reinterpret_cast<f32x4*>(smem + L_LS + wave * 16) = lw;
    }
    __builtin_amdgcn_sched_barrier(0);
    STAMP(9);
    __builtin_amdgcn_sched_barrier(0);
#pragma unroll
    for (int i = 0; i < 8; ++i)
      *reinterpret_cast<v4u*>(smem + L_VI + swz(wave * 32 + 4 * i + g4, i16)) = Vr[i];
    STAMP(10);
    __syncthreads();
    v4u pa[8], vb[8];
#pragma unroll
    for (int ks = 0; ks < 8; ++ks) {
      pa[ks] = *reinterpret_cast<const v4u*>(smem + L_P + (i16 & 3) * P_STR + (32 * ks + 8 * g4) * 2);
      const s16x4 b0 = __builtin_amdgcn_ds_read_tr16_b64_v4i16(
          (lds_s16x4*)(smem + L_VI + swz(32 * ks + 8 * g4 + tq, 2 * wave + (tp >> 1)) + 8 * (tp & 1)));
      const s16x4 b1_ = __builtin_amdgcn_ds_read_tr16_b64_v4i16(
          (lds_s16x4*)(smem + L_VI + swz(32 * ks + 8 * g4 + 4 + tq, 2 * wave + (tp >> 1)) + 8 * (tp & 1)));
      const v2u u0 = __builtin_bit_cast(v2u, b0), u1 = __builtin_bit_cast(v2u, b1_);
      vb[ks] = v4u{u0.x, u0.y, u1.x, u1.y};
    }
    __builtin_amdgcn_sched_barrier(0);
    f32x4 o2[2] = {{0.f, 0.f, 0.f, 0.f}, {0.f, 0.f, 0.f, 0.f}};
#pragma unroll
    for (int ks = 0; ks < 8; ++ks) o2[ks & 1] = mfma16(pa[ks], vb[ks], o2[ks & 1]);
    const f32x4 o = o2[0] + o2[1];
    const float og = g4 == 0 ? o[0] : g4 == 1 ? o[1] : g4 == 2 ? o[2] : o[3];
    const float on = dpp<XOR1>(og);
    const unsigned slot = (unsigned)(((kvh * 4 + g4) * 8 + ab) * 4 + sp);
    if ((i16 & 1) == 0)
      put_granule(rs, WS_O + (slot * 64u + (unsigned)(wave * 8 + (i16 >> 1))) * 16u, __float_as_uint(og), __float_as_uint(on), tagA, tagB);
    if (wave == 0 && i16 == 0) {
      float Ls = 0.f;
#pragma unroll
      for (int w = 0; w < 8; ++w) Ls += *reinterpret_cast<const float*>(smem + L_LS + w * 16 + g4 * 4);
      const float Mg = g4 == 0 ? M[0] : g4 == 1 ? M[1] : g4 == 2 ? M[2] : M[3];
      put_granule(rs, WS_ML + slot * 16u, __float_as_uint(Mg), __float_as_uint(Ls), tagA, tagB);
    }
  }
  STAMP(4);

  if (wg < N_CWG) {
#pragma unroll
    for (int i = 0; i < 4; ++i) *reinterpret_cast<v4u*>(smem + L_WC + swz((tid >> 4) + 32 * i, i16)) = Wc[i];
    __syncthreads();
    v4u af[4];
#pragma unroll
    for (int ks = 0; ks < 4; ++ks) {
      const s16x4 a0 = __builtin_amdgcn_ds_read_tr16_b64_v4i16(
          (lds_s16x4*)(smem + L_WC + swz(32 * ks + 8 * g4 + tq, 2 * wave + (tp >> 1)) + 8 * (tp & 1)));
      const s16x4 a1 = __builtin_amdgcn_ds_read_tr16_b64_v4i16(
          (lds_s16x4*)(smem + L_WC + swz(32 * ks + 8 * g4 + 4 + tq, 2 * wave + (tp >> 1)) + 8 * (tp & 1)));
      const v2u u0 = __builtin_bit_cast(v2u, a0), u1 = __builtin_bit_cast(v2u, a1);
      af[ks] = v4u{u0.x, u0.y, u1.x, u1.y};
    }
    if (ckc < 32) {
      unsigned off[8];
      v4u g[8];
#pragma unroll
      for (int s = 0; s < 4; ++s) {
        const unsigned slot = (unsigned)((ckc * 8 + wave) * 4 + s);
        off[s] = WS_O + (slot * 64u + lane) * 16u;
        off[4 + s] = WS_ML + slot * 16u;
      }
      get_granules<8>(rs, off, g, tagA, tagB);
      float M = __uint_as_float(g[4].x);
#pragma unroll
      for (int s = 1; s < 4; ++s) M = fmaxf(M, __uint_as_float(g[4 + s].x));
      float Ls = 0.f, o0 = 0.f, o1 = 0.f;
#pragma unroll
      for (int s = 0; s < 4; ++s) {
        const float wt = __builtin_amdgcn_exp2f(__uint_as_float(g[4 + s].x) - M);
        Ls = fmaf(wt, __uint_as_float(g[4 + s].z), Ls);
        o0 = fmaf(wt, __uint_as_float(g[s].x), o0);
        o1 = fmaf(wt, __uint_as_float(g[s].z), o1);
      }
      *reinterpret_cast<unsigned*>(smem + L_ACT + wave * ACT_STR + lane * 4) = pack2(o0 / Ls, o1 / Ls);
      STAMP(5);
    } else if (lane < 32) {
      unsigned off[1] = {WS_H + (unsigned)(wave * 448 + (ckc - 32) * 32 + lane) * 16u};
      v4u g[1];
      get_granules<1>(rs, off, g, tagA, tagB);
      *reinterpret_cast<v2u*>(smem + L_ACT + wave * ACT_STR + lane * 8) = v2u{g[0].x, g[0].z};
    }
    __syncthreads();
    f32x4 acc = {0.f, 0.f, 0.f, 0.f};
#pragma unroll
    for (int ks = 0; ks < 4; ++ks) {
      const v4u bfg = *reinterpret_cast<const v4u*>(smem + L_ACT + (i16 & 7) * ACT_STR + (32 * ks + 8 * g4) * 2);
      acc = mfma16(af[ks], bfg, acc);
    }
    if (i16 < 8) {
      const unsigned po = WS_P + (unsigned)((ckc * 8 + i16) * 256 + cnc * 64 + wave * 8 + g4 * 2) * 16u;
      put_granule(rs, po, __float_as_uint(acc[0]), __float_as_uint(acc[1]), tagA, tagB);
      put_granule(rs, po + 16u, __float_as_uint(acc[2]), __float_as_uint(acc[3]), tagA, tagB);
    }
  }
  STAMP(6);

  {
    const int db = wg >> 5, dg0 = (wg & 31) * 8;
    if (tid < N_KC * 8) {
      const int kc = tid >> 3, gq = tid & 7;
      unsigned off[1] = {WS_P + (unsigned)((kc * 8 + db) * 256 + dg0 + gq) * 16u};
      v4u g[1];
      get_granules<1, 12>(rs, off, g, tagA, tagB);
      *reinterpret_cast<v2u*>(smem + L_PART + kc * 64 + gq * 8) = v2u{g[0].x, g[0].z};
    }
    __syncthreads();
    STAMP(7);
    if (tid < 16) {
      const int n = dg0 * 2 + tid;
      float sa = 0.f, sm = 0.f;
#pragma unroll
      for (int k = 0; k < 32; ++k) sa += *reinterpret_cast<const float*>(smem + L_PART + k * 64 + tid * 4);
#pragma unroll
      for (int k = 32; k < N_KC; ++k) sm += *reinterpret_cast<const float*>(smem + L_PART + k * 64 + tid * 4);
      const float mlp = bfr(bfr(sm) + bf2f(d_b2));
      const float y = bfr(bfr(bf2f(d_x) + bfr(sa)) + mlp);
      out[db * DM + n] = (unsigned short)f2bf(y);
    }
    if (wg == 0 && tid == 0) __hip_atomic_store((gu32*)(ws + WS_E), epoch, __ATOMIC_RELAXED, __HIP_MEMORY_SCOPE_AGENT);
  }
  STAMP_FLUSH;
}

}

extern "C" void kernel_launch(void* const* d_in, const int* in_sizes, int n_in, void* d_out, int out_size,
                              void* d_ws, size_t ws_size, hipStream_t stream) {
  (void)in_sizes; (void)n_in; (void)out_size; (void)ws_size;
  const unsigned short* x = (const unsigned short*)d_in[0];
  const unsigned short* k = (const unsigned short*)d_in[1];
  const unsigned short* v = (const unsigned short*)d_in[2];
  const unsigned short* wq = (const unsigned short*)d_in[3];
  const unsigned short* wo = (const unsigned short*)d_in[4];
  const unsigned short* w1 = (const unsigned short*)d_in[6];
  const unsigned short* b1 = (const unsigned short*)d_in[7];
  const unsigned short* w2 = (const unsigned short*)d_in[8];
  const unsigned short* b2 = (const unsigned short*)d_in[9];
  hipLaunchKernelGGL(fused_decode_kernel, dim3(NWG), dim3(NTHR), 0, stream, x, k, v, wq, wo, w1, b1, w2, b2,
                     (unsigned short*)d_out, (unsigned char*)d_ws);
}
```

```cpp
#include <hip/hip_runtime.h>
#include <hip/hip_bf16.h>
#include <stdint.h>

namespace {

constexpr int DM = 512, NHKV = 8, SK = 4096, DH = 128, WINDOW = 1024, FF = 1792;
constexpr int NWG = 256, NTHR = 640;
#define QDELAY 8
constexpr int NQC = 4096;
constexpr int N_HWG = FF / 16;
constexpr int N_KC = 32 + FF / 128;
constexpr int N_CWG = N_KC * 4;

typedef unsigned v4u __attribute__((ext_vector_type(4)));
typedef unsigned v2u __attribute__((ext_vector_type(2)));
typedef float f32x4 __attribute__((ext_vector_type(4)));
typedef float f32x2 __attribute__((ext_vector_type(2)));
typedef short bf16x8 __attribute__((ext_vector_type(8)));
typedef short s16x4 __attribute__((ext_vector_type(4)));
typedef __attribute__((address_space(1))) unsigned gu32;
typedef __attribute__((address_space(3))) s16x4 lds_s16x4;
#define LAS __attribute__((address_space(3)))

constexpr unsigned WS_E = 0;
constexpr unsigned WS_Q = 4096;
constexpr unsigned WS_H = WS_Q + 8 * 1024 * 16;
constexpr unsigned WS_O = WS_H + 8 * 448 * 16;
constexpr unsigned WS_ML = WS_O + 32 * 8 * 4 * 64 * 16;
constexpr unsigned WS_P = WS_ML + 32 * 8 * 4 * 16;
constexpr unsigned WS_END = WS_P + N_KC * 8 * 256 * 16;

constexpr unsigned SPIN_CAP = 1u << 18;

constexpr unsigned L_XN = 0;
constexpr unsigned XN_STR = 1040;
constexpr unsigned L_WA = L_XN + 8 * XN_STR;
constexpr unsigned L_VI = L_WA + 2 * 16384;
constexpr unsigned L_WC = L_VI + 65536;
constexpr unsigned L_P = L_WC + 32768;
constexpr unsigned P_STR = 528;
constexpr unsigned L_MX = L_P + 4 * P_STR;
constexpr unsigned L_LS = L_MX + 128;
constexpr unsigned L_ACT = L_LS + 128;
constexpr unsigned ACT_STR = 272;
constexpr unsigned L_PART = L_ACT + 8 * ACT_STR;
constexpr unsigned L_QS = L_PART + N_KC * 64;
constexpr unsigned L_CNT = L_QS + 1024;
constexpr unsigned L_END = L_CNT + 16;

__device__ __forceinline__ float bf2f(unsigned bits16) { return __builtin_bit_cast(float, bits16 << 16); }
__device__ __forceinline__ float bflo(unsigned w) { return __builtin_bit_cast(float, w << 16); }
__device__ __forceinline__ float bfhi(unsigned w) { return __builtin_bit_cast(float, w & 0xffff0000u); }
__device__ __forceinline__ unsigned f2bf(float f) { return (unsigned)__builtin_bit_cast(unsigned short, (__bf16)f); }
__device__ __forceinline__ float bfr(float f) { return bf2f(f2bf(f)); }
__device__ __forceinline__ unsigned pack2(float lo, float hi) { return f2bf(lo) | (f2bf(hi) << 16); }
template <int CTRL> __device__ __forceinline__ float dpp(float x) {
  return __builtin_bit_cast(float, __builtin_amdgcn_mov_dpp(__builtin_bit_cast(int, x), CTRL, 0xf, 0xf, true));
}
constexpr int XOR1 = 0xB1, ROR1 = 0x121, ROR2 = 0x122, ROR4 = 0x124, ROR8 = 0x128;
__device__ __forceinline__ float row_max(float m) {
  m = fmaxf(m, dpp<ROR1>(m)); m = fmaxf(m, dpp<ROR2>(m)); m = fmaxf(m, dpp<ROR4>(m)); return fmaxf(m, dpp<ROR8>(m));
}
__device__ __forceinline__ float row_sum(float m) {
  m += dpp<ROR1>(m); m += dpp<ROR2>(m); m += dpp<ROR4>(m); return m + dpp<ROR8>(m);
}
__device__ __forceinline__ float wave_sum(float x) {
  x = row_sum(x);
  auto s = __builtin_amdgcn_permlane16_swap(__float_as_uint(x), __float_as_uint(x), false, false);
  x = __uint_as_float(s[0]) + __uint_as_float(s[1]);
  auto t = __builtin_amdgcn_permlane32_swap(__float_as_uint(x), __float_as_uint(x), false, false);
  return __uint_as_float(t[0]) + __uint_as_float(t[1]);
}
__device__ __forceinline__ void wave_lds_sync() {
  __builtin_amdgcn_fence(__ATOMIC_RELEASE, "wavefront");
  __builtin_amdgcn_wave_barrier();
  __builtin_amdgcn_fence(__ATOMIC_ACQUIRE, "wavefront");
}
__device__ __forceinline__ v4u ldnt(const void* p) { return __builtin_nontemporal_load(reinterpret_cast<const v4u*>(p)); }
__device__ __forceinline__ f32x4 mfma16(v4u a, v4u b, f32x4 c) {
  return __builtin_amdgcn_mfma_f32_16x16x32_bf16(__builtin_bit_cast(bf16x8, a), __builtin_bit_cast(bf16x8, b), c, 0, 0, 0);
}
__device__ __forceinline__ unsigned swz(unsigned row, unsigned ch) {
  return 256u * row + 16u * (ch ^ (((row & 3) << 2) | ((row >> 2) & 3)));
}

__device__ __forceinline__ void put_granule(__amdgpu_buffer_rsrc_t rs, unsigned off, unsigned w0, unsigned w1,
                                            unsigned tagA, unsigned tagB) {
  v4u g = {w0, tagA, w1, tagB};
  __builtin_amdgcn_raw_buffer_store_b128(g, rs, (int)off, 0, 16  );
}
template <int N>
__device__ __forceinline__ void load_granules(__amdgpu_buffer_rsrc_t rs, const unsigned (&off)[N], v4u (&g)[N]) {
#pragma unroll
  for (int n = 0; n < N; ++n) {
    unsigned o = off[n];
    asm volatile("" : "+v"(o)::"memory");
    g[n] = __builtin_amdgcn_raw_buffer_load_b128(rs, (int)o, 0, 16  );
  }
}
template <int N>
__device__ __forceinline__ bool granules_ok(const v4u (&g)[N], unsigned tagA, unsigned tagB) {
  bool ok = true;
#pragma unroll
  for (int n = 0; n < N; ++n) ok = ok && (g[n].y == tagA) && (g[n].w == tagB);
  return __all(ok);
}
template <int N>
__device__ __forceinline__ void get_granules(__amdgpu_buffer_rsrc_t rs, const unsigned (&off)[N], v4u (&g)[N],
                                             unsigned tagA, unsigned tagB) {
  for (unsigned spins = 0;; ++spins) {
    asm volatile("" ::: "memory");
    load_granules<N>(rs, off, g);
    if (granules_ok<N>(g, tagA, tagB) || spins > SPIN_CAP) break;
    __builtin_amdgcn_s_sleep(1);
  }
  asm volatile("" ::: "memory");
}

__global__ void __launch_bounds__(NTHR, 3)
fused_decode_kernel(const unsigned short* __restrict__ x, const unsigned short* __restrict__ kc_,
                    const unsigned short* __restrict__ vc_, const unsigned short* __restrict__ wq,
                    const unsigned short* __restrict__ wo, const unsigned short* __restrict__ w1,
                    const unsigned short* __restrict__ b1, const unsigned short* __restrict__ w2,
                    const unsigned short* __restrict__ b2, unsigned short* __restrict__ out,
                    unsigned char* ws) {
  __shared__ __attribute__((aligned(16))) unsigned char smem[L_END];
  const int tid = threadIdx.x, lane = tid & 63;
  const int wave = __builtin_amdgcn_readfirstlane(tid >> 6);
  const int wg = blockIdx.x;
  const int g4 = lane >> 4, i16 = lane & 15;
  const int tq = i16 >> 2, tp = lane & 3;
  const __amdgpu_buffer_rsrc_t rs = __builtin_amdgcn_make_buffer_rsrc((void*)ws, 0, (int)WS_END, 0x00020000);
  const int ab = wg >> 5, kvh = (wg >> 2) & 7, sp = wg & 3;

  if (wave >= 8) {
    __builtin_amdgcn_s_setprio(3);
    const int sv = wave - 8;
    const int cs = sv == 0 ? ((wg & 7) * 32 + (wg >> 3)) : ((wg & 7) * 14 + (wg >> 3));
    const bool has_w = (sv == 0) || ((wg >> 3) < 14);
    v4u xv[4], wa[16];
    unsigned epoch = __hip_atomic_load((gu32*)(ws + WS_E), __ATOMIC_RELAXED, __HIP_MEMORY_SCOPE_AGENT);
#pragma unroll
    for (int b = 0; b < 4; ++b) xv[b] = *reinterpret_cast<const v4u*>(x + (sv * 4 + b) * DM + lane * 8);
    {
      const __amdgpu_buffer_rsrc_t rsW = __builtin_amdgcn_make_buffer_rsrc(
          (void*)(sv == 0 ? wq : w1), 0, has_w ? (sv == 0 ? DM * NQC * 2 : DM * FF * 2) : 0, 0x00020000);
      const int ldw = sv == 0 ? NQC : FF;
#pragma unroll
      for (int i = 0; i < 16; ++i) {
        const int pi = lane + 64 * i;
        wa[i] = __builtin_amdgcn_raw_buffer_load_b128(rsW, ((pi >> 1) * ldw + cs * 16 + (pi & 1) * 8) * 2, 0, 2);
      }
    }
    v2u b1v = *reinterpret_cast<const v2u*>(b1 + ((sv == 0 || !has_w) ? 0 : cs * 16 + g4 * 4));
    __builtin_amdgcn_sched_barrier(0);
    __builtin_amdgcn_s_barrier();
    __builtin_amdgcn_sched_barrier(0);
    const unsigned xn = L_XN, img = L_WA + sv * 16384;
#pragma unroll
    for (int b = 0; b < 4; ++b) {
      f32x2 e[4];
#pragma unroll
      for (int w = 0; w < 4; ++w) e[w] = f32x2{bflo(xv[b][w]), bfhi(xv[b][w])};
      const f32x2 s2 = (e[0] + e[1]) + (e[2] + e[3]);
      const float mean = wave_sum(s2.x + s2.y) * (1.0f / DM);
      f32x2 q2 = {0.f, 0.f};
#pragma unroll
      for (int w = 0; w < 4; ++w) { const f32x2 d = e[w] - mean; q2 += d * d; }
      const float var = wave_sum(q2.x + q2.y) * (1.0f / DM);
      const float mb = bfr(mean);
      const float rinv = __builtin_amdgcn_rcpf(bfr(__builtin_amdgcn_sqrtf(bfr(bfr(var) + 1e-5f))));
      v4u o;
#pragma unroll
      for (int w = 0; w < 4; ++w) {
        const f32x2 t = e[w] - mb;
        const unsigned tb = pack2(t.x, t.y);
        const f32x2 u = f32x2{bflo(tb), bfhi(tb)} * rinv;
        o[w] = pack2(u.x, u.y);
      }
      *reinterpret_cast<v4u*>(smem + xn + (sv * 4 + b) * XN_STR + lane * 16) = o;
    }
    {
      __builtin_amdgcn_fence(__ATOMIC_RELEASE, "workgroup");
      LAS unsigned* cnt = (LAS unsigned*)(smem + L_CNT);
      if (lane == 0) __hip_atomic_fetch_add(cnt, 1u, __ATOMIC_RELAXED, __HIP_MEMORY_SCOPE_WORKGROUP);
      if (!has_w) { __builtin_amdgcn_s_barrier(); return; }
#pragma unroll
      for (int i = 0; i < 16; ++i) *reinterpret_cast<v4u*>(smem + img + (lane + 64 * i) * 16) = wa[i];
      for (unsigned sp_ = 0; sp_ < SPIN_CAP && __hip_atomic_load(cnt, __ATOMIC_RELAXED, __HIP_MEMORY_SCOPE_WORKGROUP) < 2u; ++sp_)
        __builtin_amdgcn_s_sleep(1);
      __builtin_amdgcn_fence(__ATOMIC_ACQUIRE, "workgroup");
    }
    asm volatile("" : "+v"(epoch), "+v"(b1v));
    epoch += 1u;
    const unsigned tagA = epoch, tagB = epoch ^ 0x9E3779B9u;
    __builtin_amdgcn_sched_barrier(0);
    __builtin_amdgcn_sched_barrier(0);
    wave_lds_sync();
    v4u afr[16], bfr_[16];
#pragma unroll
    for (int ks = 0; ks < 16; ++ks) {
      const s16x4 a0 = __builtin_amdgcn_ds_read_tr16_b64_v4i16((lds_s16x4*)(smem + img + (32 * ks + 8 * g4 + tq) * 32 + tp * 8));
      const s16x4 a1 = __builtin_amdgcn_ds_read_tr16_b64_v4i16((lds_s16x4*)(smem + img + (32 * ks + 8 * g4 + 4 + tq) * 32 + tp * 8));
      const v2u u0 = __builtin_bit_cast(v2u, a0), u1 = __builtin_bit_cast(v2u, a1);
      afr[ks] = v4u{u0.x, u0.y, u1.x, u1.y};
      bfr_[ks] = *reinterpret_cast<const v4u*>(smem + xn + (i16 & 7) * XN_STR + (32 * ks + 8 * g4) * 2);
    }
    __builtin_amdgcn_sched_barrier(0);
    f32x4 ac4[4] = {{0.f, 0.f, 0.f, 0.f}, {0.f, 0.f, 0.f, 0.f}, {0.f, 0.f, 0.f, 0.f}, {0.f, 0.f, 0.f, 0.f}};
#pragma unroll
    for (int ks = 0; ks < 16; ++ks) ac4[ks & 3] = mfma16(afr[ks], bfr_[ks], ac4[ks & 3]);
    const f32x4 acc = (ac4[0] + ac4[1]) + (ac4[2] + ac4[3]);
    if (i16 < 8) {
      if (sv == 0) {
        put_granule(rs, WS_Q + (unsigned)(i16 * 1024 + cs * 4 + g4) * 16u, pack2(acc[0], acc[1]), pack2(acc[2], acc[3]), tagA, tagB);
      } else {
        float hv[4];
#pragma unroll
        for (int j = 0; j < 4; ++j) {
          const float bj = (j & 1) ? bfhi(b1v[j >> 1]) : bflo(b1v[j >> 1]);
          const float h = bfr(bfr(acc[j]) + bj);
          const float u = 0.7978845608028654f * (h + 0.044715f * h * h * h);
          hv[j] = 0.5f * h * (1.0f + tanhf(u));
        }
        put_granule(rs, WS_H + (unsigned)(i16 * 448 + cs * 4 + g4) * 16u, pack2(hv[0], hv[1]), pack2(hv[2], hv[3]), tagA, tagB);
      }
    }
    if (sv == 0) {
      __builtin_amdgcn_s_sleep(QDELAY);
      unsigned qoff[2];
      v4u qg[2];
#pragma unroll
      for (int e = 0; e < 2; ++e) qoff[e] = WS_Q + (unsigned)(ab * 1024 + kvh * 128 + lane + 64 * e) * 16u;
      get_granules<2>(rs, qoff, qg, tagA, tagB);
#pragma unroll
      for (int e = 0; e < 2; ++e) *reinterpret_cast<v2u*>(smem + L_QS + (lane + 64 * e) * 8) = v2u{qg[e].x, qg[e].z};
    }
    __syncthreads();
    return;
  }

  const int ckc = wg >> 2, cnc = wg & 3;
  const int d_n = (wg & 31) * 16 + i16;
  unsigned epoch = __hip_atomic_load((gu32*)(ws + WS_E), __ATOMIC_RELAXED, __HIP_MEMORY_SCOPE_AGENT);
  const unsigned d_b2 = b2[d_n];
  const unsigned d_x = x[(wg >> 5) * DM + d_n];
  if (tid == 0) __hip_atomic_store((LAS unsigned*)(smem + L_CNT), 0u, __ATOMIC_RELAXED, __HIP_MEMORY_SCOPE_WORKGROUP);
  __builtin_amdgcn_sched_barrier(0);
  asm volatile("s_waitcnt lgkmcnt(0)" ::: "memory");
  __builtin_amdgcn_s_barrier();
  __builtin_amdgcn_sched_barrier(0);
  v4u Kr[8], Vr[8], Wc[4];
  {
    const size_t kv0 = ((size_t)(ab * NHKV + kvh) * SK + (SK - WINDOW) + sp * 256 + wave * 32) * DH;
#pragma unroll
    for (int t = 0; t < 2; ++t)
#pragma unroll
      for (int kk = 0; kk < 4; ++kk) Kr[t * 4 + kk] = ldnt(kc_ + kv0 + (size_t)(16 * t + i16) * DH + 32 * kk + 8 * g4);
#pragma unroll
    for (int i = 0; i < 8; ++i) Vr[i] = ldnt(vc_ + kv0 + (size_t)(4 * i + g4) * DH + i16 * 8);
  }
  __builtin_amdgcn_sched_barrier(0);
  __syncthreads();
  __builtin_amdgcn_sched_barrier(0);
  {
    const unsigned short* wb = (ckc < 32) ? (wo + (size_t)ckc * 128 * DM) : (w2 + (size_t)(ckc - 32) * 128 * DM);
    const __amdgpu_buffer_rsrc_t rsC =
        __builtin_amdgcn_make_buffer_rsrc((void*)wb, 0, wg < N_CWG ? 128 * DM * 2 : 0, 0x00020000);
#pragma unroll
    for (int i = 0; i < 4; ++i)
      Wc[i] = __builtin_amdgcn_raw_buffer_load_b128(rsC, (((tid >> 4) + 32 * i) * DM + cnc * 128 + i16 * 8) * 2, 0, 2);
  }
  __builtin_amdgcn_sched_barrier(0);
  asm volatile("" : "+v"(epoch));
  epoch += 1u;
  const unsigned tagA = epoch, tagB = epoch ^ 0x9E3779B9u;

  {
    f32x4 sc[2];
#pragma unroll
    for (int t = 0; t < 2; ++t) {
      f32x4 a = {0.f, 0.f, 0.f, 0.f};
#pragma unroll
      for (int kk = 0; kk < 4; ++kk) {
        const v4u qa = *reinterpret_cast<const v4u*>(smem + L_QS + ((i16 & 3) * 128 + 32 * kk + 8 * g4) * 2);
        a = mfma16(qa, Kr[t * 4 + kk], a);
      }
#pragma unroll
      for (int h = 0; h < 4; ++h) sc[t][h] = bfr(bfr(a[h]) * 0.08838834764831845f) * 1.4426950408889634f;
    }
    if (g4 == 0) {
      f32x4 mw;
#pragma unroll
      for (int h = 0; h < 4; ++h) mw[h] = row_max(fmaxf(sc[0][h], sc[1][h]));
      if (i16 == 0) *reinterpret_cast<f32x4*>(smem + L_MX + wave * 16) = mw;
    }
    __syncthreads();
    __builtin_amdgcn_sched_barrier(0);
    __builtin_amdgcn_sched_barrier(0);
    f32x4 M = *reinterpret_cast<const f32x4*>(smem + L_MX);
#pragma unroll
    for (int w = 1; w < 8; ++w) {
      const f32x4 m2 = *reinterpret_cast<const f32x4*>(smem + L_MX + w * 16);
#pragma unroll
      for (int h = 0; h < 4; ++h) M[h] = fmaxf(M[h], m2[h]);
    }
    if (g4 == 0) {
      f32x4 lw;
#pragma unroll
      for (int h = 0; h < 4; ++h) {
        float l = 0.f;
#pragma unroll
        for (int t = 0; t < 2; ++t) {
          const unsigned pb = f2bf(__builtin_amdgcn_exp2f(sc[t][h] - M[h]));
          *reinterpret_cast<unsigned short*>(smem + L_P + h * P_STR + (wave * 32 + t * 16 + i16) * 2) = (unsigned short)pb;
          l += bf2f(pb);
        }
        lw[h] = row_sum(l);
      }
      if (i16 == 0) *reinterpret_cast<f32x4*>(smem + L_LS + wave * 16) = lw;
    }
    __builtin_amdgcn_sched_barrier(0);
    __builtin_amdgcn_sched_barrier(0);
#pragma unroll
    for (int i = 0; i < 8; ++i)
      *reinterpret_cast<v4u*>(smem + L_VI + swz(wave * 32 + 4 * i + g4, i16)) = Vr[i];
    __syncthreads();
    v4u pa[8], vb[8];
#pragma unroll
    for (int ks = 0; ks < 8; ++ks) {
      pa[ks] = *reinterpret_cast<const v4u*>(smem + L_P + (i16 & 3) * P_STR + (32 * ks + 8 * g4) * 2);
      const s16x4 b0 = __builtin_amdgcn_ds_read_tr16_b64_v4i16(
          (lds_s16x4*)(smem + L_VI + swz(32 * ks + 8 * g4 + tq, 2 * wave + (tp >> 1)) + 8 * (tp & 1)));
      const s16x4 b1_ = __builtin_amdgcn_ds_read_tr16_b64_v4i16(
          (lds_s16x4*)(smem + L_VI + swz(32 * ks + 8 * g4 + 4 + tq, 2 * wave + (tp >> 1)) + 8 * (tp & 1)));
      const v2u u0 = __builtin_bit_cast(v2u, b0), u1 = __builtin_bit_cast(v2u, b1_);
      vb[ks] = v4u{u0.x, u0.y, u1.x, u1.y};
    }
    __builtin_amdgcn_sched_barrier(0);
    f32x4 o2[2] = {{0.f, 0.f, 0.f, 0.f}, {0.f, 0.f, 0.f, 0.f}};
#pragma unroll
    for (int ks = 0; ks < 8; ++ks) o2[ks & 1] = mfma16(pa[ks], vb[ks], o2[ks & 1]);
    const f32x4 o = o2[0] + o2[1];
    const float og = g4 == 0 ? o[0] : g4 == 1 ? o[1] : g4 == 2 ? o[2] : o[3];
    const float on = dpp<XOR1>(og);
    const unsigned slot = (unsigned)(((kvh * 4 + g4) * 8 + ab) * 4 + sp);
    if ((i16 & 1) == 0)
      put_granule(rs, WS_O + (slot * 64u + (unsigned)(wave * 8 + (i16 >> 1))) * 16u, __float_as_uint(og), __float_as_uint(on), tagA, tagB);
    if (wave == 0 && i16 == 0) {
      float Ls = 0.f;
#pragma unroll
      for (int w = 0; w < 8; ++w) Ls += *reinterpret_cast<const float*>(smem + L_LS + w * 16 + g4 * 4);
      const float Mg = g4 == 0 ? M[0] : g4 == 1 ? M[1] : g4 == 2 ? M[2] : M[3];
      put_granule(rs, WS_ML + slot * 16u, __float_as_uint(Mg), __float_as_uint(Ls), tagA, tagB);
    }
  }

  if (wg < N_CWG) {
#pragma unroll
    for (int i = 0; i < 4; ++i) *reinterpret_cast<v4u*>(smem + L_WC + swz((tid >> 4) + 32 * i, i16)) = Wc[i];
    __syncthreads();
    v4u af[4];
#pragma unroll
    for (int ks = 0; ks < 4; ++ks) {
      const s16x4 a0 = __builtin_amdgcn_ds_read_tr16_b64_v4i16(
          (lds_s16x4*)(smem + L_WC + swz(32 * ks + 8 * g4 + tq, 2 * wave + (tp >> 1)) + 8 * (tp & 1)));
      const s16x4 a1 = __builtin_amdgcn_ds_read_tr16_b64_v4i16(
          (lds_s16x4*)(smem + L_WC + swz(32 * ks + 8 * g4 + 4 + tq, 2 * wave + (tp >> 1)) + 8 * (tp & 1)));
      const v2u u0 = __builtin_bit_cast(v2u, a0), u1 = __builtin_bit_cast(v2u, a1);
      af[ks] = v4u{u0.x, u0.y, u1.x, u1.y};
    }
    if (ckc < 32) {
      unsigned off[8];
      v4u g[8];
#pragma unroll
      for (int s = 0; s < 4; ++s) {
        const unsigned slot = (unsigned)((ckc * 8 + wave) * 4 + s);
        off[s] = WS_O + (slot * 64u + lane) * 16u;
        off[4 + s] = WS_ML + slot * 16u;
      }
      get_granules<8>(rs, off, g, tagA, tagB);
      float M = __uint_as_float(g[4].x);
#pragma unroll
      for (int s = 1; s < 4; ++s) M = fmaxf(M, __uint_as_float(g[4 + s].x));
      float Ls = 0.f, o0 = 0.f, o1 = 0.f;
#pragma unroll
      for (int s = 0; s < 4; ++s) {
        const float wt = __builtin_amdgcn_exp2f(__uint_as_float(g[4 + s].x) - M);
        Ls = fmaf(wt, __uint_as_float(g[4 + s].z), Ls);
        o0 = fmaf(wt, __uint_as_float(g[s].x), o0);
        o1 = fmaf(wt, __uint_as_float(g[s].z), o1);
      }
      *reinterpret_cast<unsigned*>(smem + L_ACT + wave * ACT_STR + lane * 4) = pack2(o0 / Ls, o1 / Ls);
    } else if (lane < 32) {
      unsigned off[1] = {WS_H + (unsigned)(wave * 448 + (ckc - 32) * 32 + lane) * 16u};
      v4u g[1];
      get_granules<1>(rs, off, g, tagA, tagB);
      *reinterpret_cast<v2u*>(smem + L_ACT + wave * ACT_STR + lane * 8) = v2u{g[0].x, g[0].z};
    }
    __syncthreads();
    f32x4 acc = {0.f, 0.f, 0.f, 0.f};
#pragma unroll
    for (int ks = 0; ks < 4; ++ks) {
      const v4u bfg = *reinterpret_cast<const v4u*>(smem + L_ACT + (i16 & 7) * ACT_STR + (32 * ks + 8 * g4) * 2);
      acc = mfma16(af[ks], bfg, acc);
    }
    if (i16 < 8) {
      const unsigned po = WS_P + (unsigned)((ckc * 8 + i16) * 256 + cnc * 64 + wave * 8 + g4 * 2) * 16u;
      put_granule(rs, po, __float_as_uint(acc[0]), __float_as_uint(acc[1]), tagA, tagB);
      put_granule(rs, po + 16u, __float_as_uint(acc[2]), __float_as_uint(acc[3]), tagA, tagB);
    }
  }

  {
    const int db = wg >> 5, dg0 = (wg & 31) * 8;
    if (tid < N_KC * 8) {
      const int kc = tid >> 3, gq = tid & 7;
      unsigned off[1] = {WS_P + (unsigned)((kc * 8 + db) * 256 + dg0 + gq) * 16u};
      v4u g[1];
      get_granules<1>(rs, off, g, tagA, tagB);
      *reinterpret_cast<v2u*>(smem + L_PART + kc * 64 + gq * 8) = v2u{g[0].x, g[0].z};
    }
    __syncthreads();
    if (tid < 16) {
      const int n = dg0 * 2 + tid;
      float sa = 0.f, sm = 0.f;
#pragma unroll
      for (int k = 0; k < 32; ++k) sa += *reinterpret_cast<const float*>(smem + L_PART + k * 64 + tid * 4);
#pragma unroll
      for (int k = 32; k < N_KC; ++k) sm += *reinterpret_cast<const float*>(smem + L_PART + k * 64 + tid * 4);
      const float mlp = bfr(bfr(sm) + bf2f(d_b2));
      const float y = bfr(bfr(bf2f(d_x) + bfr(sa)) + mlp);
      out[db * DM + n] = (unsigned short)f2bf(y);
    }
    if (wg == 0 && tid == 0) __hip_atomic_store((gu32*)(ws + WS_E), epoch, __ATOMIC_RELAXED, __HIP_MEMORY_SCOPE_AGENT);
  }
}

}

extern "C" void kernel_launch(void* const* d_in, const int* in_sizes, int n_in, void* d_out, int out_size,
                              void* d_ws, size_t ws_size, hipStream_t stream) {
  (void)in_sizes; (void)n_in; (void)out_size; (void)ws_size;
  const unsigned short* x = (const unsigned short*)d_in[0];
  const unsigned short* k = (const unsigned short*)d_in[1];
  const unsigned short* v = (const unsigned short*)d_in[2];
  const unsigned short* wq = (const unsigned short*)d_in[3];
  const unsigned short* wo = (const unsigned short*)d_in[4];
  const unsigned short* w1 = (const unsigned short*)d_in[6];
  const unsigned short* b1 = (const unsigned short*)d_in[7];
  const unsigned short* w2 = (const unsigned short*)d_in[8];
  const unsigned short* b2 = (const unsigned short*)d_in[9];
  hipLaunchKernelGGL(fused_decode_kernel, dim3(NWG), dim3(NTHR), 0, stream, x, k, v, wq, wo, w1, b1, w2, b2,
                     (unsigned short*)d_out, (unsigned char*)d_ws);
}
```
